```python
import torch

def _softmax(x, axis=-1):
    return torch.softmax(x, dim=axis)
def _gelu(x):
    return torch.nn.functional.gelu(x, approximate="tanh")
def _silu(x):
    return torch.nn.functional.silu(x)
def _sigmoid(x):
    return torch.sigmoid(x)
def _layernorm(x, axis=-1, eps=1e-5):
    m = x.mean(dim=axis, keepdim=True)
    v = x.var(dim=axis, keepdim=True, unbiased=False)
    return (x - m) / torch.sqrt(v + eps)
def _scatter_add(tgt, idx, src, axis=0):
    return tgt.clone().index_add_(axis, idx, src)
def _segment_sum(src, seg, n):
    out = torch.zeros((n,) + src.shape[1:], dtype=src.dtype)
    return out.index_add_(0, seg, src)
def _pad(x, pw):
    flat = tuple(v for pair in reversed(pw) for v in pair)
    return torch.nn.functional.pad(x, flat)

kernel_name = 'concat_heads__mha_block_cached__mha_block_cached__near_ridge'

def setup_inputs():

    _w = (lambda a: (lambda t: t.to(torch.bfloat16) if t.is_floating_point() else t)(torch.as_tensor(a)))
    def _A_setup():
        B, Sq, D = 8, 128, 640
        H, Sk, Dh = 32, 1024, 192
        return {
            "x": _w((lambda *s: torch.randn(*s, dtype=torch.bfloat16) * 0.8)(B, Sq, D)),
            "k_cache": _w((lambda *s: torch.randn(*s, dtype=torch.bfloat16) * 0.8)(B, H, Sk, Dh)),
            "v_cache": _w((lambda *s: torch.randn(*s, dtype=torch.bfloat16) * 0.05)(B, H, Sk, Dh)),
            "wq": _w((lambda *s: torch.randn(*s, dtype=torch.bfloat16) * 0.8)(D, H * Dh) * (D ** -0.5)),
            "wo": _w((lambda *s: torch.randn(*s, dtype=torch.bfloat16) * 0.05)(H * Dh, D) * ((H * Dh) ** -0.5)),
        }
    _A_in = _A_setup()

    def _B_setup():
        B, Sq, D = 8, 128, 640
        H, Sk, Dh = 32, 1024, 192
        return {
            "x": _w((lambda *s: torch.randn(*s, dtype=torch.bfloat16) * 0.8)(B, Sq, D)),
            "k_cache": _w((lambda *s: torch.randn(*s, dtype=torch.bfloat16) * 0.8)(B, H, Sk, Dh)),
            "v_cache": _w((lambda *s: torch.randn(*s, dtype=torch.bfloat16) * 0.05)(B, H, Sk, Dh)),
            "wq": _w((lambda *s: torch.randn(*s, dtype=torch.bfloat16) * 0.8)(D, H * Dh) * (D ** -0.5)),
            "wo": _w((lambda *s: torch.randn(*s, dtype=torch.bfloat16) * 0.05)(H * Dh, D) * ((H * Dh) ** -0.5)),
        }
    _B_in = _B_setup()

    return {
        "A_x": _A_in["x"],
        "A_k_cache": _A_in["k_cache"],
        "A_v_cache": _A_in["v_cache"],
        "A_wq": _A_in["wq"],
        "A_wo": _A_in["wo"],
        "B_x": _B_in["x"],
        "B_k_cache": _B_in["k_cache"],
        "B_v_cache": _B_in["v_cache"],
        "B_wq": _B_in["wq"],
        "B_wo": _B_in["wo"],
    }

def reference(A_x, A_k_cache, A_v_cache, A_wq, A_wo, B_x, B_k_cache, B_v_cache, B_wq, B_wo):
    def _A_ref(x, k_cache, v_cache, wq, wo):
        B, Sq, D = x.shape
        H, Dh = 32, 192
        q = (lambda x, *d: x.permute(*d))((x @ wq).reshape(B, Sq, H, Dh), 0, 2, 1, 3)
        scores = torch.einsum("bhqd,bhkd->bhqk", q, k_cache) * (Dh ** -0.5)
        p = _softmax(scores, axis=-1)
        o = torch.einsum("bhqk,bhkd->bhqd", p, v_cache)
        return (lambda x, *d: x.permute(*d))(o, 0, 2, 1, 3).reshape(B, Sq, H * Dh) @ wo

    def _B_ref(x, k_cache, v_cache, wq, wo):
        B, Sq, D = x.shape
        H, Dh = 32, 192
        q = (lambda x, *d: x.permute(*d))((x @ wq).reshape(B, Sq, H, Dh), 0, 2, 1, 3)
        scores = torch.einsum("bhqd,bhkd->bhqk", q, k_cache) * (Dh ** -0.5)
        p = _softmax(scores, axis=-1)
        o = torch.einsum("bhqk,bhkd->bhqd", p, v_cache)
        return (lambda x, *d: x.permute(*d))(o, 0, 2, 1, 3).reshape(B, Sq, H * Dh) @ wo

    a = _A_ref(A_x, A_k_cache, A_v_cache, A_wq, A_wo)
    b = _B_ref(B_x, B_k_cache, B_v_cache, B_wq, B_wo)
    return torch.concatenate([a, b], axis=-1)
```

```cpp
#include <hip/hip_runtime.h>
#include <hip/hip_bf16.h>

#define DI __device__ __forceinline__
typedef __bf16 bf16x8 __attribute__((ext_vector_type(8)));
typedef __bf16 bf16x2 __attribute__((ext_vector_type(2)));
typedef short s16x4 __attribute__((ext_vector_type(4)));
typedef short s16x8 __attribute__((ext_vector_type(8)));
typedef float f32x2 __attribute__((ext_vector_type(2)));
typedef float f32x4 __attribute__((ext_vector_type(4)));
typedef float f32x16 __attribute__((ext_vector_type(16)));
typedef unsigned u32x4 __attribute__((ext_vector_type(4)));
typedef __amdgpu_buffer_rsrc_t srd_t;
#define LAS __attribute__((address_space(3)))
#define GAS __attribute__((address_space(1)))

#define HEAD_ROT 1
#define POL_HX " nt"
#define POL_HW " nt"
#define POL_X2 " nt"
#define POL_W2 " nt"
#define POL_WO " nt"

constexpr unsigned ITEM = 1024u * 192u * 2u;
constexpr unsigned KV_BYTES = 256u * ITEM, X_BYTES = 8u * 128u * 640u * 2u, W_BYTES = 640u * 6144u * 2u, OUT_BYTES = 1024u * 1280u * 2u;
constexpr unsigned WS_FLAG_BYTES = 32768u, WS_O = 1u << 20, O_BYTES = 512u * 49152u, WS_P = WS_O + O_BYTES, P_BYTES = 256u * 40960u;
constexpr unsigned WS_STAMP = WS_P + P_BYTES;
constexpr unsigned L_K = 0, L_V = 36864, L_G = 73728, L_QX = 81920, LDS_BYTES = 163840;
constexpr float CS = 0.07216878364870322f * 1.4426950408889634f;
constexpr float THR = 6.0f;
constexpr unsigned SPIN_CAP = 1u << 18;

struct Params { const void *x[2], *k[2], *v[2], *wq[2], *wo[2]; void* out; void* ws; };

DI srd_t mk_srd(const void* p, unsigned bytes) { return __builtin_amdgcn_make_buffer_rsrc((void*)p, (short)0, (int)bytes, 0x00020000); }
DI f32x16 mfma(bf16x8 a, bf16x8 b, f32x16 c) { return __builtin_amdgcn_mfma_f32_32x32x16_bf16(a, b, c, 0, 0, 0); }
DI unsigned cvtpk(float lo, float hi) { f32x2 v = {lo, hi}; return __builtin_bit_cast(unsigned, __builtin_convertvector(v, bf16x2)); }
template <int S> DI bf16x8 pack8(const f32x16& x) {
  u32x4 w = {cvtpk(x[8 * S], x[8 * S + 1]), cvtpk(x[8 * S + 2], x[8 * S + 3]), cvtpk(x[8 * S + 4], x[8 * S + 5]), cvtpk(x[8 * S + 6], x[8 * S + 7])};
  return __builtin_bit_cast(bf16x8, w);
}
DI bf16x8 lds_rd(unsigned a) { return *(const LAS bf16x8*)(size_t)a; }
DI void lds_wr(unsigned a, bf16x8 v) { *(LAS bf16x8*)(size_t)a = v; }
DI bf16x8 lds_tr(unsigned a0, unsigned a1) {
  s16x4 lo = __builtin_amdgcn_ds_read_tr16_b64_v4i16((LAS s16x4*)(size_t)a0), hi = __builtin_amdgcn_ds_read_tr16_b64_v4i16((LAS s16x4*)(size_t)a1);
  s16x8 r = __builtin_shufflevector(lo, hi, 0, 1, 2, 3, 4, 5, 6, 7);
  return __builtin_bit_cast(bf16x8, r);
}
template <int N> DI void waitvm() { asm volatile("s_waitcnt vmcnt(%0)" ::"i"(N) : "memory"); }
DI void bar() { asm volatile("s_waitcnt lgkmcnt(0)\n\ts_barrier" ::: "memory"); }
#define SBAR() __builtin_amdgcn_sched_barrier(0)

#define DMA3(POL, m0v, v0, v1, v2, srd, so) \
  asm volatile("s_mov_b32 m0, %0\n\ts_nop 0\n\tbuffer_load_dwordx4 %1, %4, %5 offen" POL " lds\n\tbuffer_load_dwordx4 %2, %4, %5 offen offset:1024" POL " lds\n\t" \
               "buffer_load_dwordx4 %3, %4, %5 offen offset:2048" POL " lds" ::"s"(m0v), "v"(v0), "v"(v1), "v"(v2), "s"(srd), "s"(so) : "memory")
#define DMA2(POL, m0v, v0, v1, srd, so0, so1) \
  asm volatile("s_mov_b32 m0, %0\n\ts_nop 0\n\tbuffer_load_dwordx4 %1, %3, %4 offen" POL " lds\n\tbuffer_load_dwordx4 %2, %3, %5 offen offset:1024" POL " lds" \
               ::"s"(m0v), "v"(v0), "v"(v1), "s"(srd), "s"(so0), "s"(so1) : "memory")
#define DMA5(POL, m0v, m0w, vo, srd, so) \
  asm volatile("s_mov_b32 m0, %0\n\ts_nop 0\n\tbuffer_load_dwordx4 %2, %7, %8 offen" POL " lds\n\tbuffer_load_dwordx4 %3, %7, %8 offen offset:1024" POL " lds\n\t" \
               "buffer_load_dwordx4 %4, %7, %8 offen offset:2048" POL " lds\n\tbuffer_load_dwordx4 %5, %7, %8 offen offset:3072" POL " lds\n\t" \
               "s_mov_b32 m0, %1\n\ts_nop 0\n\tbuffer_load_dwordx4 %6, %7, %8 offen" POL " lds" \
               ::"s"(m0v), "s"(m0w), "v"(vo[0]), "v"(vo[1]), "v"(vo[2]), "v"(vo[3]), "v"(vo[4]), "s"(srd), "s"(so) : "memory")
DI void st_sc1(bf16x8 v, unsigned voff, srd_t s, unsigned so) { asm volatile("buffer_store_dwordx4 %0, %1, %2, %3 offen sc1\n\ts_nop 1" ::"v"(v), "v"(voff), "s"(s), "s"(so) : "memory"); }
DI void ld4_sc1(bf16x8 (&r)[4], unsigned voff, srd_t s, unsigned so) {
  asm volatile("buffer_load_dwordx4 %0, %4, %5, %6 offen sc1\n\tbuffer_load_dwordx4 %1, %4, %5, %6 offen offset:1024 sc1\n\t"
               "buffer_load_dwordx4 %2, %4, %5, %6 offen offset:2048 sc1\n\tbuffer_load_dwordx4 %3, %4, %5, %6 offen offset:3072 sc1"
               : "=&v"(r[0]), "=&v"(r[1]), "=&v"(r[2]), "=&v"(r[3]) : "v"(voff), "s"(s), "s"(so) : "memory");
}
template <int N> DI void waitvm_pin4(bf16x8 (&r)[4]) { asm volatile("s_waitcnt vmcnt(%4)" : "+v"(r[0]), "+v"(r[1]), "+v"(r[2]), "+v"(r[3]) : "i"(N) : "memory"); }

DI float max3f(float a, float b, float c) { return __builtin_fmaxf(__builtin_fmaxf(a, b), c); }
DI float rowmax(const f32x16& s) {
  float a = max3f(s[0], s[1], s[2]), b = max3f(s[3], s[4], s[5]);
  a = max3f(a, s[6], s[7]); b = max3f(b, s[8], s[9]); a = max3f(a, s[10], s[11]); b = max3f(b, s[12], s[13]); a = max3f(a, s[14], s[15]);
  const float m = __builtin_fmaxf(a, b);
  auto rr = __builtin_amdgcn_permlane32_swap(__float_as_uint(m), __float_as_uint(m), false, false);
  return __builtin_fmaxf(__uint_as_float(rr[0]), __uint_as_float(rr[1]));
}
DI void arrive_wait(GAS unsigned* ctr, unsigned want, unsigned lane) {
  if (lane == 0) __hip_atomic_fetch_add(ctr, 1u, __ATOMIC_RELAXED, __HIP_MEMORY_SCOPE_AGENT);
  for (unsigned s = 0; s < SPIN_CAP; ++s) {
    if ((unsigned)__builtin_amdgcn_readfirstlane(__hip_atomic_load(ctr, __ATOMIC_RELAXED, __HIP_MEMORY_SCOPE_AGENT)) >= want) break;
    __builtin_amdgcn_s_sleep(1);
  }
}

#define STAMP_DECL
#define STAMP_AT(i)

struct AttnState { f32x16 O[6]; bf16x8 Q[12]; float mneg, l; unsigned kb[4], vb[2]; };

#define PIN(x) asm volatile("" : "+v"(x))
DI bf16x8 k_rd(const AttnState& a, int i) { return lds_rd(a.kb[i & 3] + 128 * (i >> 2)); }
DI bf16x8 v_rd(const AttnState& a, int i) { const int t = i % 6; const unsigned ad = a.vb[t & 1] + 128 * (t >> 1) + 6144 * (i / 6); return lds_tr(ad, ad + 3072); }
DI f32x16 qk(const AttnState& a) {
  f32x16 s = {};
  bf16x8 kf[3] = {k_rd(a, 0), k_rd(a, 1), k_rd(a, 2)};
  SBAR();
#pragma unroll
  for (int i = 0; i < 12; ++i) {
    s = mfma(kf[i % 3], a.Q[i], s);
    if (i + 3 < 12) kf[i % 3] = k_rd(a, i + 3);
    SBAR();
  }
  return s;
}
template <bool QK> DI float step_body(AttnState& a, f32x16& sc, f32x16& sn) {
  bf16x8 kf[3], vf[3];
  u32x4 p0, p1;
  if (QK) { kf[0] = k_rd(a, 0); kf[1] = k_rd(a, 1); kf[2] = k_rd(a, 2); }
  SBAR();
#define EX2(r) do { sc[r] = __builtin_amdgcn_exp2f(__builtin_fmaf(sc[r], CS, a.mneg)); sc[r + 1] = __builtin_amdgcn_exp2f(__builtin_fmaf(sc[r + 1], CS, a.mneg)); } while (0)
  EX2(0); EX2(2); PIN(sc);
  SBAR();
#pragma unroll
  for (int i = 0; i < 12; ++i) {
    if (QK) {
      sn = mfma(kf[i % 3], a.Q[i], i ? sn : f32x16{});
      if (i + 3 < 12) kf[i % 3] = k_rd(a, i + 3);
    }
    if (i < 6) { EX2(4 + 2 * i); PIN(sc); }
    if (i >= 6 && i < 8) { p0[2 * i - 12] = cvtpk(sc[4 * i - 24], sc[4 * i - 23]); p0[2 * i - 11] = cvtpk(sc[4 * i - 22], sc[4 * i - 21]); PIN(p0); }
    if (i >= 8 && i < 10) { p1[2 * i - 16] = cvtpk(sc[4 * i - 24], sc[4 * i - 23]); p1[2 * i - 15] = cvtpk(sc[4 * i - 22], sc[4 * i - 21]); PIN(p1); }
    if (i >= 9) vf[i - 9] = v_rd(a, i - 9);
    SBAR();
  }
#undef EX2
  float s0, s1, m0, m1;
#pragma unroll
  for (int i = 0; i < 12; ++i) {
    a.O[i % 6] = mfma(vf[i % 3], __builtin_bit_cast(bf16x8, i < 6 ? p0 : p1), a.O[i % 6]);
    if (i + 3 < 12) vf[i % 3] = v_rd(a, i + 3);
    if (i == 0) { s0 = sc[0] + sc[1]; s1 = sc[2] + sc[3]; s0 += sc[4]; s1 += sc[5]; PIN(s0); PIN(s1); }
    if (i >= 1 && i < 3) { s0 += sc[4 * i + 2]; s1 += sc[4 * i + 3]; s0 += sc[4 * i + 4]; s1 += sc[4 * i + 5]; PIN(s0); PIN(s1); }
    if (i == 3) { s0 += sc[14]; s1 += sc[15]; a.l += s0 + s1; PIN(a.l); }
    if (QK) {
      if (i == 4) { m0 = max3f(sn[0], sn[1], sn[2]); m1 = max3f(sn[3], sn[4], sn[5]); m0 = max3f(m0, sn[6], sn[7]); m1 = max3f(m1, sn[8], sn[9]); PIN(m0); PIN(m1); }
      if (i == 5) { m0 = max3f(m0, sn[10], sn[11]); m1 = max3f(m1, sn[12], sn[13]); m0 = max3f(m0, sn[14], sn[15]); m0 = __builtin_fmaxf(m0, m1); PIN(m0); }
      if (i == 6) { auto rr = __builtin_amdgcn_permlane32_swap(__float_as_uint(m0), __float_as_uint(m0), false, false);
                    m0 = __builtin_fmaxf(__uint_as_float(rr[0]), __uint_as_float(rr[1])); PIN(m0); }
    }
    SBAR();
  }
  return QK ? m0 : 0.f;
}
DI void advance(AttnState& a, unsigned& slot) {
  const int dk = slot == 1 ? -24576 : 12288, dv = slot == 2 ? -24576 : 12288;
#pragma unroll
  for (int i = 0; i < 4; ++i) a.kb[i] += dk;
  a.vb[0] += dv; a.vb[1] += dv;
  slot = slot == 2 ? 0 : slot + 1;
}
DI void attn_step(AttnState& a, f32x16& sc, f32x16& sn, unsigned& slot) {
  bar();
  const float g = __builtin_fmaf(step_body<true>(a, sc, sn), CS, a.mneg);
  if (__builtin_expect(__any(g > THR), 0)) {
    const float dl = __builtin_fmaxf(g, 0.f), f = __builtin_amdgcn_exp2f(-dl);
    a.mneg -= dl; a.l *= f;
#pragma unroll
    for (int t = 0; t < 6; ++t) a.O[t] *= f;
  }
  advance(a, slot);
}

template <int R> DI void attn_item(AttnState& a, f32x16& sA, unsigned& slot, const unsigned qo, const srd_t sOW, const unsigned so) {
  f32x16 sB;
  a.mneg = -CS * rowmax(sA); a.l = 0.f;
#pragma unroll
  for (int t = 0; t < 6; ++t) a.O[t] = f32x16{};
#pragma nounroll
  for (int k = 0; k < 15; ++k) { attn_step(a, sA, sB, slot); attn_step(a, sB, sA, slot); }
  attn_step(a, sA, sB, slot);
  bar();
  if (R == 0) {
#pragma unroll
    for (int f = 0; f < 12; ++f) a.Q[f] = lds_rd(qo + 1024 * f);
    step_body<true>(a, sB, sA);
  } else step_body<false>(a, sB, sA);
  advance(a, slot);
  auto rr = __builtin_amdgcn_permlane32_swap(__float_as_uint(a.l), __float_as_uint(a.l), false, false);
  const float inv = 1.0f / (__uint_as_float(rr[0]) + __uint_as_float(rr[1]));
  unsigned l16 = 0;
  if (R == 1) { asm volatile("v_mbcnt_lo_u32_b32 %0, -1, 0\n\tv_mbcnt_hi_u32_b32 %0, -1, %0" : "=v"(l16)); l16 *= 16; }
#pragma unroll
  for (int t = 0; t < 6; ++t) {
    const f32x16 o = a.O[t] * inv;
    const bf16x8 f0 = pack8<0>(o), f1 = pack8<1>(o);
    if (R == 0) { lds_wr(qo + 2048 * t, f0); lds_wr(qo + 2048 * t + 1024, f1); }
    else { st_sc1(f0, l16, sOW, so + 2048 * t); st_sc1(f1, l16, sOW, so + 2048 * t + 1024); }
    SBAR();
  }
}

DI void tail(const Params& p, const unsigned L, const unsigned wv, const unsigned blk) {
  unsigned lane;
  asm volatile("v_mbcnt_lo_u32_b32 %0, -1, 0\n\tv_mbcnt_hi_u32_b32 %0, -1, %0" : "=v"(lane));
  const unsigned w = wv & 3, half = wv >> 2, xcd = blk & 7, j = blk >> 3, br = xcd >> 2, hg = xcd & 3, b = j >> 2, hh = j & 3;
  const unsigned r32 = lane & 31, hi = lane >> 5, g1 = (lane >> 4) & 1, q4 = (lane & 15) >> 2, p4 = lane & 3, sp4 = ((p4 & 1) << 1) | (p4 >> 1);
  const srd_t sOW = mk_srd((char*)p.ws + WS_O, O_BYTES);
  const srd_t sO = mk_srd(br ? p.wo[1] : p.wo[0], W_BYTES), sPW = mk_srd((char*)p.ws + WS_P, P_BYTES);
  GAS unsigned* flags = (GAS unsigned*)p.ws;
  unsigned vO[5];
#pragma unroll
  for (int q = 0; q < 5; ++q) { const unsigned pp = 64 * q + lane; vO[q] = (pp / 20) * 1280 + 16 * (pp % 20) - (q < 4 ? 1024 * q : 0); }
  const unsigned hbase = 16 * (1 - half) + 4 * hg;
  const unsigned tb = L + 81920 * half;
  auto wo_issue = [&](int s) {
    const unsigned m = tb + 20480 * (s & 3) + 5120 * w;
    DMA5(POL_WO, m, m + 4096, vO, sO, (hbase * 192 + 64 * s + 16 * w) * 1280 + hh * 320);
  };
  const unsigned obase = ((br * 8 + b) * 32 + hbase) * 49152 + 12288 * w;
  bar();
  wo_issue(0); wo_issue(1); wo_issue(2);
  waitvm<15>();
  bar();
  if (wv == 0) arrive_wait(flags + 64 * ((br * 4 + hg) * 8 + b), 4, lane);
  bar();
  STAMP_AT(4);
  f32x16 acc[5] = {};
  {
    bf16x8 of[3][4];
    ld4_sc1(of[0], 16 * lane, sOW, obase);
    ld4_sc1(of[1], 16 * lane, sOW, obase + 4096);
    const unsigned wob = tb + 320 * (4 * hi + q4) + 32 * g1 + 8 * sp4;
    waitvm_pin4<4>(of[0]);
#pragma unroll
    for (int s = 0; s < 12; ++s) {
      bar();
      if (s + 3 < 12) wo_issue(s + 3);
      if (s + 2 < 12) ld4_sc1(of[(s + 2) % 3], 16 * lane, sOW, obase + ((s + 2) / 3) * 49152 + ((s + 2) % 3) * 4096);
#pragma unroll
      for (int k = 0; k < 4; ++k)
#pragma unroll
        for (int t = 0; t < 5; ++t) {
          const unsigned ad = wob + 20480 * (s & 3) + 5120 * k + 64 * t;
          acc[t] = mfma(lds_tr(ad, ad + 2560), of[s % 3][k], acc[t]);
        }
      if (s + 3 < 12) waitvm_pin4<9>(of[(s + 1) % 3]);
      else if (s + 2 < 12) waitvm_pin4<4>(of[(s + 1) % 3]);
      else if (s + 1 < 12) waitvm_pin4<0>(of[(s + 1) % 3]);
    }
  }
  STAMP_AT(5);
  bar();
  const unsigned cb = L + 81920 + 20480 * w + 16 * lane;
  if (half) {
#pragma unroll
    for (int t = 0; t < 5; ++t)
#pragma unroll
      for (int g = 0; g < 4; ++g) *(LAS f32x4*)(size_t)(cb + 4096 * t + 1024 * g) = f32x4{acc[t][4 * g], acc[t][4 * g + 1], acc[t][4 * g + 2], acc[t][4 * g + 3]};
  }
  bar();
  const unsigned pgrp = (br * 8 + b) * 4 + hh;
  if (!half) {
#pragma unroll
    for (int t = 0; t < 5; ++t) {
#pragma unroll
      for (int g = 0; g < 4; ++g) {
        const f32x4 v = *(const LAS f32x4*)(size_t)(cb + 4096 * t + 1024 * g);
#pragma unroll
        for (int e = 0; e < 4; ++e) acc[t][4 * g + e] += v[e];
      }
      const unsigned so = (pgrp * 4 + hg) * 40960 + 10240 * w + 2048 * t;
      st_sc1(pack8<0>(acc[t]), 16 * lane, sPW, so); st_sc1(pack8<1>(acc[t]), 16 * lane, sPW, so + 1024);
    }
    waitvm<0>();
  }
  bar();
  if (wv == 0) arrive_wait(flags + 64 * (64 + pgrp), 4, lane);
  bar();
  STAMP_AT(6);
  {
    const srd_t sOut = mk_srd(p.out, OUT_BYTES);
    for (unsigned u = wv; u < 10; u += 8) {
      bf16x8 pr[4];
      const unsigned so = pgrp * 4 * 40960 + 10240 * hg + 1024 * u;
      asm volatile("buffer_load_dwordx4 %0, %4, %5, %6 offen sc1\n\tbuffer_load_dwordx4 %1, %4, %5, %7 offen sc1\n\t"
                   "buffer_load_dwordx4 %2, %4, %5, %8 offen sc1\n\tbuffer_load_dwordx4 %3, %4, %5, %9 offen sc1\n\ts_waitcnt vmcnt(0)"
                   : "=&v"(pr[0]), "=&v"(pr[1]), "=&v"(pr[2]), "=&v"(pr[3])
                   : "v"(16 * lane), "s"(sPW), "s"(so), "s"(so + 40960), "s"(so + 81920), "s"(so + 122880) : "memory");
      float sum[8];
#pragma unroll
      for (int e = 0; e < 8; ++e) sum[e] = ((float)pr[0][e] + (float)pr[1][e]) + ((float)pr[2][e] + (float)pr[3][e]);
      const u32x4 res = {cvtpk(sum[0], sum[1]), cvtpk(sum[2], sum[3]), cvtpk(sum[4], sum[5]), cvtpk(sum[6], sum[7])};
      __builtin_amdgcn_raw_buffer_store_b128(res, sOut, r32 * 2560 + 16 * hi, (b * 128 + 32 * hg) * 2560 + br * 1280 + hh * 320 + 32 * u, 0);
    }
  }
}

__global__ __launch_bounds__(512, 2) void mha2_fused(Params p) {
  extern __shared__ __attribute__((aligned(16))) char lds_[];
  STAMP_DECL;
  STAMP_AT(0);
  const unsigned tid = threadIdx.x, lane = tid & 63;
  const unsigned wv = __builtin_amdgcn_readfirstlane(tid >> 6), w = wv & 3, half = wv >> 2;
  const unsigned L = (unsigned)(size_t)lds_;
  const unsigned blk = blockIdx.x, xcd = blk & 7, j = blk >> 3, br = xcd >> 2, hg = xcd & 3, b = j >> 2, hh = j & 3;
  const unsigned h0 = 4 * hg + hh;
  const unsigned r32 = lane & 31, hi = lane >> 5, g1 = (lane >> 4) & 1, q4 = (lane & 15) >> 2, p4 = lane & 3;
  const unsigned bq = q4 >> 1, sp4 = ((p4 & 1) << 1) | (p4 >> 1);
  const srd_t sX = mk_srd(br ? p.x[1] : p.x[0], X_BYTES), sQ = mk_srd(br ? p.wq[1] : p.wq[0], W_BYTES);
  const srd_t sK = mk_srd(br ? p.k[1] : p.k[0], KV_BYTES), sV = mk_srd(br ? p.v[1] : p.v[0], KV_BYTES);
  const srd_t sOW = mk_srd((char*)p.ws + WS_O, O_BYTES);

  unsigned vK[3], vV[3], vQ[3];
#pragma unroll
  for (int q = 0; q < 3; ++q) {
    const unsigned pp = 64 * q + lane, r = pp / 24, c = pp % 24;
    vK[q] = 384 * (8 * w + r) + 16 * (c ^ (((8 * w + r) >> 1) & 7)) - 1024 * q;
    vV[q] = 384 * (8 * w + r) + 16 * (c ^ ((r & 2) << 1)) - 1024 * q;
    vQ[q] = 12288 * r + 16 * (c ^ ((r & 2) << 1)) - 1024 * q;
  }
  const unsigned kv0 = (b * 32 + h0) * ITEM;

  bf16x8 qh[6];
  {
    const unsigned vX0 = (lane >> 3) * 1280 + 16 * ((lane & 7) ^ (lane >> 4)), vX1 = (lane >> 3) * 1280 + 16 * ((lane & 7) ^ (4 + (lane >> 4)));
    unsigned gk = HEAD_ROT ? ((5 * b + 10 * hh) >> 2) % 10 : 0;
    auto issue = [&](unsigned buf) {
      const unsigned sx = (b * 128 + 16 * wv) * 1280 + gk * 128;
      DMA2(POL_HX, L + buf + 2048 * wv, vX0, vX1, sX, sx, sx + 8 * 1280 - 1024);
      DMA3(POL_HW, L + buf + 16384 + 3072 * wv, vQ[0], vQ[1], vQ[2], sQ, (gk * 64 + 8 * wv) * 12288 + h0 * 384);
      gk = gk == 9 ? 0 : gk + 1;
    };
    issue(81920); issue(122880); issue(0);
    unsigned xb[4], wb[3];
#pragma unroll
    for (int s = 0; s < 4; ++s) xb[s] = L + 81920 + 128 * (32 * w + r32) + 16 * ((2 * s + hi) ^ ((r32 >> 1) & 7));
#pragma unroll
    for (int i = 0; i < 3; ++i) { const unsigned t = 3 * half + i; wb[i] = L + 81920 + 16384 + 384 * (8 * hi + q4) + 64 * ((t & 1) ^ bq) + 128 * (t >> 1) + 32 * g1 + 8 * sp4; }
    f32x16 acc[3] = {};
    unsigned buf = 2;
    auto step = [&]() {
#pragma unroll
      for (int s = 0; s < 4; ++s) {
        const bf16x8 xf = lds_rd(xb[s]);
#pragma unroll
        for (int i = 0; i < 3; ++i) acc[i] = mfma(lds_tr(wb[i] + 6144 * s, wb[i] + 6144 * s + 1536), xf, acc[i]);
      }
      const int d = buf == 3 ? -122880 : 40960;
#pragma unroll
      for (int s = 0; s < 4; ++s) xb[s] += d;
#pragma unroll
      for (int i = 0; i < 3; ++i) wb[i] += d;
      buf = (buf + 1) & 3;
    };
    for (int g = 0; g < 7; ++g) { waitvm<10>(); bar(); issue(((buf + 3) & 3) * 40960); step(); }
    waitvm<10>(); bar();
    if (half) {
#pragma unroll
      for (int s = 0; s < 3; ++s) DMA3(" nt", L + L_K + 12288 * s + 3072 * w, vK[0], vK[1], vK[2], sK, kv0 + 12288 * s);
    }
    step();
    if (half) waitvm<14>(); else waitvm<5>();
    bar();
    if (half) {
#pragma unroll
      for (int s = 0; s < 2; ++s) DMA3(" nt", L + L_V + 12288 * s + 3072 * w, vV[0], vV[1], vV[2], sV, kv0 + 12288 * s);
    }
    step();
    if (half) waitvm<15>(); else waitvm<0>();
    bar();
    step();
#pragma unroll
    for (int i = 0; i < 3; ++i) { qh[2 * i] = pack8<0>(acc[i]); qh[2 * i + 1] = pack8<1>(acc[i]); }
  }
  STAMP_AT(1);

  if (half == 0) {
    AttnState a;
    bar();
#pragma unroll
    for (int f = 0; f < 6; ++f) { a.Q[f] = qh[f]; a.Q[6 + f] = lds_rd(L + L_QX + 6144 * w + 1024 * f + 16 * lane); }
#pragma unroll
    for (int s = 0; s < 4; ++s) a.kb[s] = L + L_K + 384 * r32 + 16 * ((2 * s + hi) ^ ((r32 >> 1) & 7));
#pragma unroll
    for (int e = 0; e < 2; ++e) a.vb[e] = L + L_V + 384 * (4 * hi + q4) + 64 * (e ^ bq) + 32 * g1 + 8 * p4;
    f32x16 sA = qk(a);
#pragma unroll
    for (int s = 0; s < 4; ++s) a.kb[s] += 12288;
    unsigned slot = 0;
    attn_item<0>(a, sA, slot, L + L_G + 12288 * w + 16 * lane, sOW, 0);
    STAMP_AT(2);
    attn_item<1>(a, sA, slot, 0, sOW, ((br * 8 + b) * 32 + h0 + 16) * 49152 + 12288 * w);
  } else {
#pragma unroll
    for (int f = 0; f < 6; ++f) lds_wr(L + L_QX + 6144 * w + 1024 * f + 16 * lane, qh[f]);
    waitvm<12>();
    bar();
    waitvm<3>();
    unsigned kslot = 0, vslot = 24576, kofs = kv0 + 3 * 12288, vofs = kv0 + 2 * 12288;
    auto kv = [&](int i) {
      kofs += i == 29 ? 15 * ITEM : 0;
      vofs += i == 30 ? 15 * ITEM : 0;
      DMA3(" nt", L + L_K + kslot + 3072 * w, vK[0], vK[1], vK[2], sK, kofs);
      DMA3(" nt", L + L_V + vslot + 3072 * w, vV[0], vV[1], vV[2], sV, vofs);
      kofs += 12288; vofs += 12288;
      kslot = kslot == 24576 ? 0 : kslot + 12288; vslot = vslot == 24576 ? 0 : vslot + 12288;
    };
    {
      const unsigned vX = (lane >> 2) * 1280 + 16 * ((lane & 3) ^ ((lane >> 4) & 3));
      unsigned sx = (b * 128 + 32 * w) * 1280, sq = 8 * w * 12288 + (h0 + 16) * 384, ibuf = 0;
      auto aux = [&]() {
        DMA2(POL_X2, L + L_G + ibuf + 2048 * w, vX, vX, sX, sx, sx + 16 * 1280 - 1024);
        DMA3(POL_W2, L + L_G + ibuf + 8192 + 3072 * w, vQ[0], vQ[1], vQ[2], sQ, sq);
        sx += 64; sq += 32 * 12288; ibuf = ibuf == 40960 ? 0 : ibuf + 20480;
      };
      unsigned xb[2], wb[2], cbuf = 0;
#pragma unroll
      for (int s = 0; s < 2; ++s) {
        xb[s] = L + L_G + 64 * (32 * w + r32) + 16 * ((2 * s + hi) ^ ((r32 >> 2) & 3));
        wb[s] = L + L_G + 8192 + 384 * (8 * hi + q4) + 64 * (s ^ bq) + 32 * g1 + 8 * sp4;
      }
      f32x16 acc[6] = {};
      auto gemm = [&]() {
#pragma unroll
        for (int s = 0; s < 2; ++s) {
          const bf16x8 xf = lds_rd(xb[s]);
#pragma unroll
          for (int t = 0; t < 6; ++t) acc[t] = mfma(lds_tr(wb[t & 1] + 128 * (t >> 1) + 6144 * s, wb[t & 1] + 128 * (t >> 1) + 6144 * s + 1536), xf, acc[t]);
        }
        const int d = cbuf == 2 ? -40960 : 20480;
        xb[0] += d; xb[1] += d; wb[0] += d; wb[1] += d;
        cbuf = cbuf == 2 ? 0 : cbuf + 1;
      };
      bar(); kv(0); waitvm<6>();
      for (int i = 1; i < 3; ++i) { bar(); aux(); kv(i); waitvm<11>(); }
      for (int i = 3; i < 21; ++i) { bar(); aux(); kv(i); gemm(); waitvm<11>(); }
      for (int i = 21; i < 23; ++i) { bar(); kv(i); gemm(); waitvm<6>(); }
      bar(); kv(23);
#pragma unroll
      for (int t = 0; t < 6; ++t) {
        lds_wr(L + L_G + 12288 * w + 2048 * t + 16 * lane, pack8<0>(acc[t]));
        lds_wr(L + L_G + 12288 * w + 2048 * t + 1024 + 16 * lane, pack8<1>(acc[t]));
      }
      waitvm<6>();
    }
    for (int i = 24; i < 32; ++i) { bar(); kv(i); waitvm<6>(); }
    STAMP_AT(2);
    bf16x8 o1[12];
    bar(); kv(32);
#pragma unroll
    for (int f = 0; f < 12; ++f) o1[f] = lds_rd(L + L_G + 12288 * w + 1024 * f + 16 * lane);
    waitvm<6>();
    for (int i = 33; i < 61; ++i) { bar(); kv(i); waitvm<6>(); }
    bar();
    DMA3(" nt", L + L_V + vslot + 3072 * w, vV[0], vV[1], vV[2], sV, vofs);
#pragma unroll
    for (int f = 0; f < 12; ++f) st_sc1(o1[f], 16 * lane, sOW, ((br * 8 + b) * 32 + h0) * 49152 + 12288 * w + 1024 * f);
    waitvm<15>();
    bar(); waitvm<12>();
    bar();
  }
  STAMP_AT(3);

  tail(p, L, wv, blk);
}

extern "C" void kernel_launch(void* const* d_in, const int* in_sizes, int n_in, void* d_out, int out_size, void* d_ws, size_t ws_size, hipStream_t stream) {
  Params p;
  for (int i = 0; i < 2; ++i) { p.x[i] = d_in[5 * i]; p.k[i] = d_in[5 * i + 1]; p.v[i] = d_in[5 * i + 2]; p.wq[i] = d_in[5 * i + 3]; p.wo[i] = d_in[5 * i + 4]; }
  p.out = d_out; p.ws = d_ws;
  (void)hipFuncSetAttribute((const void*)mha2_fused, hipFuncAttributeMaxDynamicSharedMemorySize, (int)LDS_BYTES);
  (void)hipMemsetAsync(d_ws, 0, WS_FLAG_BYTES, stream);
  hipLaunchKernelGGL(mha2_fused, dim3(256), dim3(512), LDS_BYTES, stream, p);
}
```

```cpp
#include <hip/hip_runtime.h>
#include <hip/hip_bf16.h>

#define DI __device__ __forceinline__
typedef __bf16 bf16x8 __attribute__((ext_vector_type(8)));
typedef __bf16 bf16x2 __attribute__((ext_vector_type(2)));
typedef short s16x4 __attribute__((ext_vector_type(4)));
typedef short s16x8 __attribute__((ext_vector_type(8)));
typedef float f32x2 __attribute__((ext_vector_type(2)));
typedef float f32x4 __attribute__((ext_vector_type(4)));
typedef float f32x16 __attribute__((ext_vector_type(16)));
typedef unsigned u32x2 __attribute__((ext_vector_type(2)));
typedef unsigned u32x4 __attribute__((ext_vector_type(4)));
typedef unsigned long long u64;
typedef __amdgpu_buffer_rsrc_t srd_t;
#define LAS __attribute__((address_space(3)))
#define GAS __attribute__((address_space(1)))

#define POL_SH ""
#define I_O1 55
#define I_WO 59
#define PRIO_H 0

constexpr unsigned ITEM = 1024u * 192u * 2u;
constexpr unsigned KV_BYTES = 256u * ITEM, X_BYTES = 8u * 128u * 640u * 2u, W_BYTES = 640u * 6144u * 2u, OUT_BYTES = 1024u * 1280u * 2u;
constexpr unsigned WS_EPOCH = 0, WS_FLAG = 4096, CTL_BYTES = 4096u + 3u * 256u * 128u;
constexpr unsigned WS_O = 1u << 20, O_BYTES = 512u * 49152u, WS_P = WS_O + O_BYTES, P_BYTES = 256u * 40960u;
constexpr unsigned WS_STAMP = WS_P + P_BYTES;
constexpr unsigned L_K = 0, L_V = 36864, L_G = 73728, LDS_BYTES = 163840;
constexpr unsigned wo_buf(int tile) { return (tile & 7) < 4 ? 81920u + 20480u * (tile & 7) : 20480u * ((tile & 7) - 4); }
constexpr float CS = 0.07216878364870322f * 1.4426950408889634f;
constexpr float THR = 6.0f;
constexpr unsigned SPIN_CAP = 1u << 18;

struct Params { const void *x[2], *k[2], *v[2], *wq[2], *wo[2]; void* out; void* ws; };

DI srd_t mk_srd(const void* p, unsigned bytes) { return __builtin_amdgcn_make_buffer_rsrc((void*)p, (short)0, (int)bytes, 0x00020000); }
DI f32x16 mfma(bf16x8 a, bf16x8 b, f32x16 c) { return __builtin_amdgcn_mfma_f32_32x32x16_bf16(a, b, c, 0, 0, 0); }
DI unsigned cvtpk(float lo, float hi) { f32x2 v = {lo, hi}; return __builtin_bit_cast(unsigned, __builtin_convertvector(v, bf16x2)); }
template <int S> DI bf16x8 pack8(const f32x16& x) {
  u32x4 w = {cvtpk(x[8 * S], x[8 * S + 1]), cvtpk(x[8 * S + 2], x[8 * S + 3]), cvtpk(x[8 * S + 4], x[8 * S + 5]), cvtpk(x[8 * S + 6], x[8 * S + 7])};
  return __builtin_bit_cast(bf16x8, w);
}
DI bf16x8 lds_rd(unsigned a) { return *(const LAS bf16x8*)(size_t)a; }
DI void lds_wr(unsigned a, bf16x8 v) { *(LAS bf16x8*)(size_t)a = v; }
DI f32x4 lds_rdf(unsigned a) { return *(const LAS f32x4*)(size_t)a; }
DI void lds_wrf(unsigned a, f32x4 v) { *(LAS f32x4*)(size_t)a = v; }
DI bf16x8 lds_tr(unsigned a0, unsigned a1) {
  s16x4 lo = __builtin_amdgcn_ds_read_tr16_b64_v4i16((LAS s16x4*)(size_t)a0), hi = __builtin_amdgcn_ds_read_tr16_b64_v4i16((LAS s16x4*)(size_t)a1);
  s16x8 r = __builtin_shufflevector(lo, hi, 0, 1, 2, 3, 4, 5, 6, 7);
  return __builtin_bit_cast(bf16x8, r);
}
template <int N> DI void waitvm() { asm volatile("s_waitcnt vmcnt(%0)" ::"i"(N) : "memory"); }
DI void bar() { asm volatile("s_waitcnt lgkmcnt(0)\n\ts_barrier" ::: "memory"); }
#define SBAR() __builtin_amdgcn_sched_barrier(0)
#define PIN(x) asm volatile("" : "+v"(x))
DI unsigned lane_id() { unsigned l; asm volatile("v_mbcnt_lo_u32_b32 %0, -1, 0\n\tv_mbcnt_hi_u32_b32 %0, -1, %0" : "=v"(l)); return l; }

#define DMA3(POL, m0v, v0, v1, v2, srd, so) \
  asm volatile("s_mov_b32 m0, %0\n\ts_nop 0\n\tbuffer_load_dwordx4 %1, %4, %5 offen" POL " lds\n\tbuffer_load_dwordx4 %2, %4, %5 offen offset:1024" POL " lds\n\t" \
               "buffer_load_dwordx4 %3, %4, %5 offen offset:2048" POL " lds" ::"s"(m0v), "v"(v0), "v"(v1), "v"(v2), "s"(srd), "s"(so) : "memory")
#define DMA2(POL, m0v, v0, v1, srd, so0, so1) \
  asm volatile("s_mov_b32 m0, %0\n\ts_nop 0\n\tbuffer_load_dwordx4 %1, %3, %4 offen" POL " lds\n\tbuffer_load_dwordx4 %2, %3, %5 offen offset:1024" POL " lds" \
               ::"s"(m0v), "v"(v0), "v"(v1), "s"(srd), "s"(so0), "s"(so1) : "memory")
#define DMA4X(POL, m0v, v0, v1, srd, so) \
  asm volatile("s_mov_b32 m0, %0\n\ts_nop 0\n\tbuffer_load_dwordx4 %1, %3, %4 offen" POL " lds\n\tbuffer_load_dwordx4 %2, %3, %5 offen offset:1024" POL " lds\n\t" \
               "buffer_load_dwordx4 %1, %3, %6 offen offset:2048" POL " lds\n\tbuffer_load_dwordx4 %2, %3, %7 offen offset:3072" POL " lds" \
               ::"s"(m0v), "v"(v0), "v"(v1), "s"(srd), "s"(so), "s"((so) + 9216u), "s"((so) + 18432u), "s"((so) + 27648u) : "memory")
#define DMA5(POL, m0v, vo, srd, so) \
  asm volatile("s_mov_b32 m0, %0\n\ts_nop 0\n\tbuffer_load_dwordx4 %2, %7, %8 offen" POL " lds\n\tbuffer_load_dwordx4 %3, %7, %8 offen offset:1024" POL " lds\n\t" \
               "buffer_load_dwordx4 %4, %7, %8 offen offset:2048" POL " lds\n\tbuffer_load_dwordx4 %5, %7, %8 offen offset:3072" POL " lds\n\t" \
               "s_mov_b32 m0, %1\n\ts_nop 0\n\tbuffer_load_dwordx4 %6, %7, %8 offen" POL " lds" \
               ::"s"(m0v), "s"((m0v) + 4096u), "v"(vo[0]), "v"(vo[1]), "v"(vo[2]), "v"(vo[3]), "v"(vo[4]), "s"(srd), "s"(so) : "memory")
DI void touch(unsigned& jk, unsigned voff, srd_t s, unsigned so) {
  asm volatile("buffer_load_dword %0, %1, %2, %3 offen nt\n\tbuffer_load_dword %0, %1, %2, %3 offen sc1" : "+v"(jk) : "v"(voff), "s"(s), "s"(so) : "memory");
}
constexpr unsigned OOB = 0x7fffff00u;
DI void st_sc1(bf16x8 v, unsigned voff, srd_t s, unsigned so) { asm volatile("buffer_store_dwordx4 %0, %1, %2, %3 offen sc1\n\ts_nop 1" ::"v"(v), "v"(voff), "s"(s), "s"(so) : "memory"); }
DI void ld2_sc1(bf16x8& r0, bf16x8& r1, unsigned voff, srd_t s, unsigned so) {
  asm volatile("buffer_load_dwordx4 %0, %2, %3, %4 offen sc1\n\tbuffer_load_dwordx4 %1, %2, %3, %4 offen offset:1024 sc1" : "=&v"(r0), "=&v"(r1) : "v"(voff), "s"(s), "s"(so) : "memory");
}
DI void ld12_sc1(bf16x8 (&r)[12], unsigned voff, srd_t s, unsigned so) {
#pragma unroll
  for (int f = 0; f < 12; f += 2) ld2_sc1(r[f], r[f + 1], voff, s, so + 1024 * f);
}
template <int N> DI void wait_pin12(bf16x8 (&r)[12]) {
  asm volatile("s_waitcnt vmcnt(%12)" : "+v"(r[0]), "+v"(r[1]), "+v"(r[2]), "+v"(r[3]), "+v"(r[4]), "+v"(r[5]), "+v"(r[6]), "+v"(r[7]), "+v"(r[8]), "+v"(r[9]), "+v"(r[10]), "+v"(r[11]) : "i"(N) : "memory");
}
DI void ld_epoch(u32x2& e, srd_t s, unsigned so) { asm volatile("buffer_load_dwordx2 %0, off, %1, %2 sc1" : "=v"(e) : "s"(s), "s"(so) : "memory"); }
DI u64 mk_tag(u32x2 e) {
  const u64 v = ((u64)(unsigned)__builtin_amdgcn_readfirstlane(e[1]) << 32) | (unsigned)__builtin_amdgcn_readfirstlane(e[0]);
  return v * 6364136223846793005ull + 1442695040888963407ull;
}
DI void publish(void* ws, unsigned off, u64 tag) { __hip_atomic_store((GAS u64*)((char*)ws + off), tag, __ATOMIC_RELAXED, __HIP_MEMORY_SCOPE_AGENT); }
DI void await4(void* ws, unsigned off, unsigned stride, u64 tag, unsigned lane) {
  const GAS u64* f = (const GAS u64*)((char*)ws + off + stride * (lane & 3));
#pragma nounroll
  for (unsigned s = 0; s < SPIN_CAP; ++s) {
    if (__all(__hip_atomic_load(f, __ATOMIC_RELAXED, __HIP_MEMORY_SCOPE_AGENT) == tag)) break;
    __builtin_amdgcn_s_sleep(1);
  }
  asm volatile("" ::: "memory");
}

DI float max3f(float a, float b, float c) { return __builtin_fmaxf(__builtin_fmaxf(a, b), c); }
DI float rowmax(const f32x16& s) {
  float a = max3f(s[0], s[1], s[2]), b = max3f(s[3], s[4], s[5]);
  a = max3f(a, s[6], s[7]); b = max3f(b, s[8], s[9]); a = max3f(a, s[10], s[11]); b = max3f(b, s[12], s[13]); a = max3f(a, s[14], s[15]);
  const float m = __builtin_fmaxf(a, b);
  auto rr = __builtin_amdgcn_permlane32_swap(__float_as_uint(m), __float_as_uint(m), false, false);
  return __builtin_fmaxf(__uint_as_float(rr[0]), __uint_as_float(rr[1]));
}

#define ST(i)
#define STR(i)
#define ST_IV(a)

struct AttnState { f32x16 O[6]; bf16x8 Q[12]; float mneg, l; unsigned kb[4], vb[2];
};

DI bf16x8 k_rd(const AttnState& a, int i) { return lds_rd(a.kb[i & 3] + 128 * (i >> 2)); }
DI bf16x8 v_rd(const AttnState& a, int i) { const int t = i % 6; const unsigned ad = a.vb[t & 1] + 128 * (t >> 1) + 6144 * (i / 6); return lds_tr(ad, ad + 3072); }
DI f32x16 qk(const AttnState& a) {
  f32x16 s = {};
#pragma unroll
  for (int i = 0; i < 12; ++i) s = mfma(k_rd(a, i), a.Q[i], s);
  return s;
}
template <bool QK> DI float step_body(AttnState& a, f32x16& sc, f32x16& sn) {
  bf16x8 kf[3], vf[3];
  u32x4 p0, p1;
  if (QK) { kf[0] = k_rd(a, 0); kf[1] = k_rd(a, 1); kf[2] = k_rd(a, 2); }
  SBAR();
#define EX2(r) do { sc[r] = __builtin_amdgcn_exp2f(__builtin_fmaf(sc[r], CS, a.mneg)); sc[r + 1] = __builtin_amdgcn_exp2f(__builtin_fmaf(sc[r + 1], CS, a.mneg)); } while (0)
  EX2(0); EX2(2); PIN(sc);
  SBAR();
#pragma unroll
  for (int i = 0; i < 12; ++i) {
    if (QK) {
      sn = mfma(kf[i % 3], a.Q[i], i ? sn : f32x16{});
      if (i + 3 < 12) kf[i % 3] = k_rd(a, i + 3);
    }
    if (i < 6) { EX2(4 + 2 * i); PIN(sc); }
    if (i >= 6 && i < 8) { p0[2 * i - 12] = cvtpk(sc[4 * i - 24], sc[4 * i - 23]); p0[2 * i - 11] = cvtpk(sc[4 * i - 22], sc[4 * i - 21]); PIN(p0); }
    if (i >= 8 && i < 10) { p1[2 * i - 16] = cvtpk(sc[4 * i - 24], sc[4 * i - 23]); p1[2 * i - 15] = cvtpk(sc[4 * i - 22], sc[4 * i - 21]); PIN(p1); }
    if (i >= 9) vf[i - 9] = v_rd(a, i - 9);
    SBAR();
  }
#undef EX2
  float s0, s1, m0, m1;
#pragma unroll
  for (int i = 0; i < 12; ++i) {
    a.O[i % 6] = mfma(vf[i % 3], __builtin_bit_cast(bf16x8, i < 6 ? p0 : p1), a.O[i % 6]);
    if (i + 3 < 12) vf[i % 3] = v_rd(a, i + 3);
    if (i == 0) { s0 = sc[0] + sc[1]; s1 = sc[2] + sc[3]; s0 += sc[4]; s1 += sc[5]; PIN(s0); PIN(s1); }
    if (i >= 1 && i < 3) { s0 += sc[4 * i + 2]; s1 += sc[4 * i + 3]; s0 += sc[4 * i + 4]; s1 += sc[4 * i + 5]; PIN(s0); PIN(s1); }
    if (i == 3) { s0 += sc[14]; s1 += sc[15]; a.l += s0 + s1; PIN(a.l); }
    if (QK) {
      if (i == 4) { m0 = max3f(sn[0], sn[1], sn[2]); m1 = max3f(sn[3], sn[4], sn[5]); m0 = max3f(m0, sn[6], sn[7]); m1 = max3f(m1, sn[8], sn[9]); PIN(m0); PIN(m1); }
      if (i == 5) { m0 = max3f(m0, sn[10], sn[11]); m1 = max3f(m1, sn[12], sn[13]); m0 = max3f(m0, sn[14], sn[15]); m0 = __builtin_fmaxf(m0, m1); PIN(m0); }
      if (i == 6) { auto rr = __builtin_amdgcn_permlane32_swap(__float_as_uint(m0), __float_as_uint(m0), false, false);
                    m0 = __builtin_fmaxf(__uint_as_float(rr[0]), __uint_as_float(rr[1])); PIN(m0); }
    }
    SBAR();
  }
  return QK ? m0 : 0.f;
}
DI void advance(AttnState& a, unsigned& slot) {
  const int dk = slot == 1 ? -24576 : 12288, dv = slot == 2 ? -24576 : 12288;
#pragma unroll
  for (int i = 0; i < 4; ++i) a.kb[i] += dk;
  a.vb[0] += dv; a.vb[1] += dv;
  slot = slot == 2 ? 0 : slot + 1;
}
DI void attn_step(AttnState& a, f32x16& sc, f32x16& sn, unsigned& slot) {
  bar();
  ST_IV(a);
  const float g = __builtin_fmaf(step_body<true>(a, sc, sn), CS, a.mneg);
  if (__builtin_expect(__any(g > THR), 0)) {
    const float dl = __builtin_fmaxf(g, 0.f), f = __builtin_amdgcn_exp2f(-dl);
    a.mneg -= dl; a.l *= f;
#pragma unroll
    for (int t = 0; t < 6; ++t) a.O[t] *= f;
  }
  advance(a, slot);
}
template <int R> DI void attn_item(AttnState& a, f32x16& sA, unsigned& slot, const unsigned qo) {
  f32x16 sB;
  a.mneg = -CS * rowmax(sA); a.l = 0.f;
#pragma unroll
  for (int t = 0; t < 6; ++t) a.O[t] = f32x16{};
#pragma nounroll
  for (int k = 0; k < 15; ++k) { attn_step(a, sA, sB, slot); attn_step(a, sB, sA, slot); }
  attn_step(a, sA, sB, slot);
  bar();
  ST_IV(a);
  if (R == 0) {
#pragma unroll
    for (int f = 0; f < 12; ++f) a.Q[f] = lds_rd(qo + 1024 * f);
    step_body<true>(a, sB, sA);
    advance(a, slot);
  } else step_body<false>(a, sB, sA);
}
DI float inv_l(const AttnState& a) {
  auto rr = __builtin_amdgcn_permlane32_swap(__float_as_uint(a.l), __float_as_uint(a.l), false, false);
  return 1.0f / (__uint_as_float(rr[0]) + __uint_as_float(rr[1]));
}

template <int T0, int NT, bool BAR> DI void oproj(f32x16 (&acc)[5], const unsigned wb, const bf16x8* of) {
  constexpr int N = 20 * NT;
  unsigned b[NT];
#pragma unroll
  for (int kk = 0; kk < NT; ++kk) { b[kk] = wb + wo_buf(T0 + kk); PIN(b[kk]); }
  auto rd = [&](int i) { const unsigned ad = b[i / 20] + 5120 * ((i % 20) / 5) + 64 * (i % 5); return lds_tr(ad, ad + 2560); };
  bf16x8 wf[6] = {rd(0), rd(1), rd(2), rd(3)};
  SBAR();
#pragma unroll
  for (int i = 0; i < N; ++i) {
    acc[i % 5] = mfma(wf[i % 6], of[i / 5], acc[i % 5]);
    if (i < 2) wf[4 + i] = rd(4 + i);
    if (i + 6 < N) wf[i % 6] = rd(i + 6);
    SBAR();
    if (BAR && i == N - 4) { waitvm<0>(); bar(); SBAR(); }
  }
}
DI unsigned wo_base(unsigned lane) { return 320 * (4 * (lane >> 5) + ((lane & 15) >> 2)) + 32 * ((lane >> 4) & 1) + 16 * (lane & 1) + 4 * (lane & 2); }
DI void wo_src(unsigned (&v)[5], unsigned lane) {
#pragma unroll
  for (int q = 0; q < 5; ++q) { const unsigned pp = 64 * q + lane; v[q] = (pp / 20) * 1280 + 16 * (pp % 20) - (q < 4 ? 1024 * q : 0); }
}

__global__ __launch_bounds__(512, 2) void mha2_fused(Params p) {
  extern __shared__ __attribute__((aligned(16))) char lds_[];
  ST(0);
  const unsigned tid = threadIdx.x;
  const unsigned wv = __builtin_amdgcn_readfirstlane(tid >> 6), w = wv & 3, half = wv >> 2;
  const unsigned L = (unsigned)(size_t)lds_;
  const unsigned blk = blockIdx.x, xcd = blk & 7, j = blk >> 3, br = xcd >> 2, hg = xcd & 3, b = j >> 2, hh = j & 3;
  const unsigned h0 = 4 * hg + hh;
  const unsigned grp = br * 8 + b, cu = (grp * 4 + hg) * 4 + hh;
  const unsigned kv0 = (b * 32 + h0) * ITEM;
  const unsigned ob = grp * 32 * 49152 + 12288 * w;
  auto ghead = [&](int g) { return g == 0 ? h0 + 16 : (g > 3 ? 16 : 0) + 4 * hg + ((hh + 1 + (g - 1) % 3) & 3); };
  const srd_t sOW = mk_srd((char*)p.ws + WS_O, O_BYTES), sC = mk_srd(p.ws, CTL_BYTES);
  u64 tag;
  f32x4 s[20];

  if (half == 0) {
    const unsigned lane = tid & 63;
    const unsigned r32 = lane & 31, hi = lane >> 5, g1 = (lane >> 4) & 1, q4 = (lane & 15) >> 2, p4 = lane & 3, bq = q4 >> 1;
    AttnState a;
    u32x2 ep;
    {
      const srd_t sX = mk_srd(br ? p.x[1] : p.x[0], X_BYTES), sQ = mk_srd(br ? p.wq[1] : p.wq[0], W_BYTES);
      unsigned jk = 0;
      const unsigned vx = lane < 40 ? (8 * w + lane / 5) * 1280 + (lane % 5) * 128 : OOB, sx = (b * 128 + 32 * hh) * 1280, sq = 8 * b * 12288 + h0 * 384, ix = 30 * w + lane;
      asm volatile("s_barrier" ::: "memory");
      touch(jk, lane < 24 ? ((1 + w) * 64 + lane / 3) * 12288 + (lane % 3) * 128 : OOB, sQ, sq);
      touch(jk, vx, sX, sx);
      unsigned xb[4], wb[2];
#pragma unroll
      for (int k = 0; k < 4; ++k) xb[k] = L + 81920 + 128 * (32 * w + r32) + 16 * ((2 * k + hi) ^ ((r32 >> 1) & 7));
#pragma unroll
      for (int e = 0; e < 2; ++e) wb[e] = L + 81920 + 16384 + 384 * (8 * hi + q4) + 64 * (e ^ bq) + 32 * g1 + 16 * (p4 & 1) + 4 * (p4 & 2);
      f32x16 qa[6] = {};
      bf16x8 wf[6] = {}, xf[2] = {};
      auto rd = [&](int i) { const int t = i % 6; const unsigned ad = wb[t & 1] + 128 * (t >> 1) + 6144 * (i / 6); return lds_tr(ad, ad + 1536); };
      unsigned buf = 2;
#pragma nounroll
      for (int g = 0; g < 10; ++g) {
        bar(); STR(20 + g);
        if (g == 0) {
          touch(jk, lane < 30 ? ((5 + ix / 24) * 64 + (ix % 24) / 3) * 12288 + (ix % 3) * 128 : OOB, sQ, sq);
          touch(jk, vx, sX, sx + 640);
          ld_epoch(ep, sC, WS_EPOCH + 256 * grp);
        }
        xf[0] = lds_rd(xb[0]); wf[0] = rd(0); wf[1] = rd(1); wf[2] = rd(2);
        SBAR();
#pragma unroll
        for (int i = 21; i < 24; ++i) { qa[i % 6] = mfma(wf[i % 6], xf[1], qa[i % 6]); wf[i % 6] = rd(i - 18); SBAR(); }
#pragma unroll
        for (int i = 0; i < 21; ++i) {
          qa[i % 6] = mfma(wf[i % 6], xf[(i / 6) & 1], qa[i % 6]);
          if (i % 6 == 0 && i < 18) xf[(i / 6 + 1) & 1] = lds_rd(xb[i / 6 + 1]);
          if (i + 6 < 24) wf[i % 6] = rd(i + 6);
          SBAR();
        }
        const int d = buf == 3 ? -122880 : 40960;
#pragma unroll
        for (int k = 0; k < 4; ++k) xb[k] += d;
        wb[0] += d; wb[1] += d;
        buf = (buf + 1) & 3;
      }
      bar();
#pragma unroll
      for (int i = 21; i < 24; ++i) qa[i % 6] = mfma(wf[i % 6], xf[1], qa[i % 6]);
#pragma unroll
      for (int t = 0; t < 6; ++t) { a.Q[2 * t] = pack8<0>(qa[t]); a.Q[2 * t + 1] = pack8<1>(qa[t]); }
      asm volatile("s_waitcnt vmcnt(0)" : "+v"(ep), "+v"(jk)::"memory");
      tag = mk_tag(ep);
    }
    ST(1);
#pragma unroll
    for (int k = 0; k < 4; ++k) a.kb[k] = L + L_K + 384 * r32 + 16 * ((2 * k + hi) ^ ((r32 >> 1) & 7));
#pragma unroll
    for (int e = 0; e < 2; ++e) a.vb[e] = L + L_V + 384 * (4 * hi + q4) + 64 * (e ^ bq) + 32 * g1 + 8 * p4;
    f32x16 sA = qk(a);
#pragma unroll
    for (int k = 0; k < 4; ++k) a.kb[k] += 12288;
    unsigned slot = 0;
    const unsigned qo = L + L_G + 12288 * w + 16 * lane;
    attn_item<0>(a, sA, slot, qo);
    {
      const float inv = inv_l(a);
#pragma unroll
      for (int t = 0; t < 6; ++t) { const f32x16 o = a.O[t] * inv; lds_wr(qo + 2048 * t, pack8<0>(o)); lds_wr(qo + 2048 * t + 1024, pack8<1>(o)); SBAR(); }
    }
    ST(2);
    attn_item<1>(a, sA, slot, 0);
    ST(3);
    bar();
    const unsigned ln = lane_id();
    bf16x8 of[12];
    {
      const float inv = inv_l(a);
#pragma unroll
      for (int t = 0; t < 6; ++t) {
        const f32x16 o = a.O[t] * inv;
        of[2 * t] = pack8<0>(o); of[2 * t + 1] = pack8<1>(o);
        st_sc1(of[2 * t], 16 * ln, sOW, ob + (h0 + 16) * 49152 + 2048 * t); st_sc1(of[2 * t + 1], 16 * ln, sOW, ob + (h0 + 16) * 49152 + 2048 * t + 1024);
        SBAR();
      }
    }
    ST(4);
    f32x16 acc[5] = {};
    const unsigned wb = L + wo_base(ln);
    const srd_t sO = mk_srd(br ? p.wo[1] : p.wo[0], W_BYTES);
    unsigned vO[5];
    wo_src(vO, ln);
#define WO_TILE(tile) DMA5(POL_SH, L + wo_buf(tile) + 5120 * w, vO, sO, (ghead((tile) / 3) * 192 + 64 * ((tile) % 3) + 16 * w) * 1280 + hh * 320)
    {
      constexpr int N = 60;
      unsigned bb[3];
#pragma unroll
      for (int kk = 0; kk < 3; ++kk) { bb[kk] = wb + wo_buf(kk); PIN(bb[kk]); }
      auto rd = [&](int i) { const unsigned ad = bb[i / 20] + 5120 * ((i % 20) / 5) + 64 * (i % 5); return lds_tr(ad, ad + 2560); };
      bf16x8 wf[6] = {rd(0), rd(1), rd(2), rd(3)};
      SBAR();
#pragma unroll
      for (int i = 0; i < N; ++i) {
        acc[i % 5] = mfma(wf[i % 6], of[i / 5], acc[i % 5]);
        if (i < 2) wf[4 + i] = rd(4 + i);
        if (i + 6 < N) wf[i % 6] = rd(i + 6);
        SBAR();
        if (i == N - 4) { bar(); SBAR(); }
      }
    }
    ST(5);
    ld12_sc1(of, 16 * ln, sOW, ob + ghead(2) * 49152); WO_TILE(8); WO_TILE(9); WO_TILE(10);
    wait_pin12<10>(of);
    ST(6);
    bar();
    if (tid == 0) publish(p.ws, WS_FLAG + 128 * (256 + cu), tag);
    oproj<6, 3, true>(acc, wb, of);
    ST(7);
    await4(p.ws, WS_FLAG + 128 * (256 + cu - hh), 128, tag, ln);
    ST(8);
    ld12_sc1(of, 16 * ln, sOW, ob + ghead(4) * 49152); WO_TILE(14); WO_TILE(15); WO_TILE(16);
    wait_pin12<10>(of);
    ST(9);
    bar();
    oproj<12, 3, true>(acc, wb, of);
    ST(10);
    ld12_sc1(of, 16 * ln, sOW, ob + ghead(6) * 49152); WO_TILE(20);
    wait_pin12<0>(of);
    ST(11);
    bar();
    oproj<18, 3, false>(acc, wb, of);
    ST(12);
    bar();
#pragma unroll
    for (int i = 0; i < 20; ++i) s[i] = f32x4{acc[i >> 2][4 * (i & 3)], acc[i >> 2][4 * (i & 3) + 1], acc[i >> 2][4 * (i & 3) + 2], acc[i >> 2][4 * (i & 3) + 3]} + lds_rdf(L + 20480 + 20480 * w + 1024 * i + 16 * ln);
    const srd_t sPW = mk_srd((char*)p.ws + WS_P, P_BYTES);
    if (w == hg) {
#pragma unroll
      for (int i = 0; i < 20; ++i) lds_wrf(L + 122880 + 1024 * i + 16 * ln, s[i]);
    } else {
#pragma unroll
      for (int u = 0; u < 10; ++u) {
        const u32x4 v = {cvtpk(s[2 * u][0], s[2 * u][1]), cvtpk(s[2 * u][2], s[2 * u][3]), cvtpk(s[2 * u + 1][0], s[2 * u + 1][1]), cvtpk(s[2 * u + 1][2], s[2 * u + 1][3])};
        st_sc1(__builtin_bit_cast(bf16x8, v), 16 * ln, sPW, ((grp * 4 + hh) * 4 + hg) * 40960 + 10240 * w + 1024 * u);
      }
      waitvm<0>();
    }
  } else {
    const unsigned lane = tid & 63;
    const srd_t sX = mk_srd(br ? p.x[1] : p.x[0], X_BYTES), sQ = mk_srd(br ? p.wq[1] : p.wq[0], W_BYTES);
    const unsigned vX0 = (lane >> 3) * 1280 + 16 * ((lane & 7) ^ (lane >> 4)), vX1 = (lane >> 3) * 1280 + 16 * ((lane & 7) ^ (4 + (lane >> 4)));
    unsigned vK[3], vV[3], vQ[3];
#pragma unroll
    for (int q = 0; q < 3; ++q) { const unsigned pp = 64 * q + lane, r = pp / 24, c = pp % 24; vQ[q] = 12288 * r + 16 * (c ^ ((r & 2) << 1)) - 1024 * q; }
    {
      unsigned sx = (b * 128 + 32 * w) * 1280, sq = 16 * w * 12288 + h0 * 384;
#define HEAD_TILE(POL, buf) do { DMA4X(POL, L + (buf) + 4096 * w, vX0, vX1, sX, sx); DMA3(POL, L + (buf) + 16384 + 6144 * w, vQ[0], vQ[1], vQ[2], sQ, sq); \
                                 DMA3(POL, L + (buf) + 19456 + 6144 * w, vQ[0], vQ[1], vQ[2], sQ, sq + 8 * 12288); sx += 128; sq += 64 * 12288; } while (0)
      HEAD_TILE(" nt", 81920);
      asm volatile("s_barrier" ::: "memory");
      HEAD_TILE(" nt", 122880);
      ST(19);
      waitvm<10>(); bar(); ST(20);
      HEAD_TILE(POL_SH, 0); HEAD_TILE(POL_SH, 40960);
      unsigned nb = 81920;
#pragma nounroll
      for (int g = 1; g < 7; ++g) { waitvm<20>(); bar(); STR(20 + g); HEAD_TILE(POL_SH, nb); nb = nb == 122880 ? 0 : nb + 40960; }
#undef HEAD_TILE
    }
    const srd_t sK = mk_srd(br ? p.k[1] : p.k[0], KV_BYTES), sV = mk_srd(br ? p.v[1] : p.v[0], KV_BYTES);
#pragma unroll
    for (int q = 0; q < 3; ++q) {
      const unsigned pp = 64 * q + lane, r = pp / 24, c = pp % 24;
      vK[q] = 384 * (8 * w + r) + 16 * (c ^ (((8 * w + r) >> 1) & 7)) - 1024 * q;
      vV[q] = 384 * (8 * w + r) + 16 * (c ^ ((r & 2) << 1)) - 1024 * q;
    }
    waitvm<20>(); bar(); ST(27);
#pragma unroll
    for (int k = 0; k < 3; ++k) DMA3(" nt", L + L_K + 12288 * k + 3072 * w, vK[0], vK[1], vK[2], sK, kv0 + 12288 * k);
    waitvm<19>(); bar(); ST(28);
#pragma unroll
    for (int k = 0; k < 2; ++k) DMA3(" nt", L + L_V + 12288 * k + 3072 * w, vV[0], vV[1], vV[2], sV, kv0 + 12288 * k);
    waitvm<15>(); bar(); ST(29);
    waitvm<12>(); bar();
    ST(1);
    waitvm<3>();
    if (PRIO_H) __builtin_amdgcn_s_setprio(PRIO_H);
    const unsigned r32 = lane & 31, hi = lane >> 5, g1 = (lane >> 4) & 1, q4 = (lane & 15) >> 2, p4 = lane & 3, bq = q4 >> 1;
    unsigned jk = 0;
    unsigned kslot = 0, vslot = 24576, kofs = kv0 + 3 * 12288, vofs = kv0 + 2 * 12288;
    auto kv = [&](int i) {
      kofs += i == 29 ? 15 * ITEM : 0;
      vofs += i == 30 ? 15 * ITEM : 0;
      DMA3(" nt", L + L_K + kslot + 3072 * w, vK[0], vK[1], vK[2], sK, kofs);
      DMA3(" nt", L + L_V + vslot + 3072 * w, vV[0], vV[1], vV[2], sV, vofs);
      kofs += 12288; vofs += 12288;
      kslot = kslot == 24576 ? 0 : kslot + 12288; vslot = vslot == 24576 ? 0 : vslot + 12288;
    };
    {
      unsigned sx = (b * 128 + 32 * w) * 1280, sq = 8 * w * 12288 + (h0 + 16) * 384, xbuf = 0, wbuf = 0;
      auto aux_x = [&]() { DMA4X(POL_SH, L + L_G + xbuf + 4096 * w, vX0, vX1, sX, sx); sx += 128; xbuf ^= 16384; };
      auto aux_w = [&]() { DMA3(POL_SH, L + L_G + 32768 + wbuf + 3072 * w, vQ[0], vQ[1], vQ[2], sQ, sq); sq += 32 * 12288; wbuf = wbuf == 24576 ? 0 : wbuf + 12288; };
      unsigned xb[4], wb[2], cbuf = 0; int xd = 16384;
#pragma unroll
      for (int k = 0; k < 4; ++k) xb[k] = L + L_G + 128 * (32 * w + r32) + 16 * ((2 * k + hi) ^ ((r32 >> 1) & 7));
#pragma unroll
      for (int e = 0; e < 2; ++e) wb[e] = L + L_G + 32768 + 384 * (8 * hi + q4) + 64 * (e ^ bq) + 32 * g1 + 16 * (p4 & 1) + 4 * (p4 & 2);
      f32x16 qa[6] = {};
      auto gemm = [&](const int odd) {
        auto rd = [&](int i) { const int t = i % 6; const unsigned ad = wb[t & 1] + 128 * (t >> 1) + 6144 * (i / 6); return lds_tr(ad, ad + 1536); };
        const bf16x8 xf0 = lds_rd(xb[2 * odd]);
        bf16x8 wf[4] = {rd(0), rd(1), rd(2), rd(3)};
        const bf16x8 xf1 = lds_rd(xb[2 * odd + 1]);
        SBAR();
#pragma unroll
        for (int i = 0; i < 12; ++i) {
          qa[i % 6] = mfma(wf[i % 4], i < 6 ? xf0 : xf1, qa[i % 6]);
          if (i + 4 < 12) wf[i % 4] = rd(i + 4);
          SBAR();
        }
        const int d = cbuf == 2 ? -24576 : 12288;
        wb[0] += d; wb[1] += d;
        cbuf = cbuf == 2 ? 0 : cbuf + 1;
        if (odd) {
#pragma unroll
          for (int k = 0; k < 4; ++k) xb[k] += xd;
          xd = -xd;
        }
      };
      { const unsigned ix = 60 * w + lane;
        bar(); kv(0); touch(jk, lane < 60 ? ((ix / 24) * 64 + (ix % 24) / 3) * 12288 + (ix % 3) * 128 : OOB, sQ, 8 * b * 12288 + (h0 + 16) * 384); waitvm<8>(); }
      bar(); kv(1); waitvm<8>();
#pragma nounroll
      for (int i = 2; i < 4; ++i) { bar(); kv(i); waitvm<6>(); }
      bar(); aux_x(); aux_w(); kv(4); waitvm<13>();
      bar(); aux_w(); kv(5); waitvm<9>();
#pragma nounroll
      for (int i = 6; i < 24; i += 2) {
        bar(); aux_x(); aux_w(); kv(i); gemm(0); waitvm<13>();
        bar(); aux_w(); kv(i + 1); gemm(1); waitvm<9>();
      }
      bar(); kv(24); gemm(0); waitvm<6>();
      bar(); kv(25); gemm(1); waitvm<6>();
      bar(); kv(26);
#pragma unroll
      for (int t = 0; t < 6; ++t) {
        lds_wr(L + L_G + 12288 * w + 2048 * t + 16 * lane, pack8<0>(qa[t]));
        lds_wr(L + L_G + 12288 * w + 2048 * t + 1024 + 16 * lane, pack8<1>(qa[t]));
      }
      waitvm<6>();
    }
    const srd_t sO = mk_srd(br ? p.wo[1] : p.wo[0], W_BYTES);
    const unsigned wsh = 8 * b * 1280 + hh * 320 - (hh & 1) * 64;
    const unsigned vW = lane < 18 ? (((18 * w + lane) / 24) * 64 + ((18 * w + lane) % 24) / 3) * 1280 + ((18 * w + lane) % 3) * 128 : OOB;
#define WO_TOUCH(head) touch(jk, vW, sO, (head) * 192 * 1280 + wsh)
    bar(); kv(27); waitvm<6>();
    bar(); kv(28); WO_TOUCH(h0); waitvm<8>();
    bar(); kv(29); waitvm<8>();
#pragma nounroll
    for (int i = 30; i < 32; ++i) { bar(); kv(i); waitvm<6>(); }
    ST(2);
    unsigned vO[5];
    wo_src(vO, lane);
    const unsigned wb = L + wo_base(lane);
    bf16x8 of[12];
    u32x2 ep;
    bar(); ld_epoch(ep, sC, WS_EPOCH + 256 * grp); kv(32);
#pragma unroll
    for (int f = 0; f < 12; ++f) of[f] = lds_rd(L + L_G + 12288 * w + 1024 * f + 16 * lane);
    waitvm<7>();
#define OWN_TILE(kk) DMA5(POL_SH, L + wo_buf(kk) + 5120 * w, vO, sO, (h0 * 192 + 64 * (kk) + 16 * w) * 1280 + hh * 320)
    bar(); OWN_TILE(0); kv(33); asm volatile("s_waitcnt vmcnt(11)" : "+v"(ep)::"memory");
    tag = mk_tag(ep);
    bar(); OWN_TILE(1); kv(34); waitvm<11>();
    bar(); OWN_TILE(2); kv(35); waitvm<11>();
    bar(); kv(36); waitvm<6>();
    f32x16 acc[5] = {};
    bar(); kv(37); oproj<0, 1, false>(acc, wb, of); waitvm<6>();
    bar(); kv(38); oproj<1, 1, false>(acc, wb, of + 4); waitvm<6>();
    bar(); kv(39); oproj<2, 1, false>(acc, wb, of + 8); waitvm<6>();
#pragma nounroll
    for (int i = 40; i < 46; ++i) { bar(); kv(i); waitvm<6>(); }
    bar(); kv(46); WO_TOUCH(h0 + 16); waitvm<8>();
    bar(); kv(47); waitvm<8>();
#pragma nounroll
    for (int i = 48; i < 50; ++i) { bar(); kv(i); waitvm<6>(); }
    bar(); WO_TILE(0); kv(50); waitvm<11>();
    bar(); WO_TILE(1); kv(51); waitvm<11>();
    bar(); WO_TILE(2); kv(52); waitvm<11>();
    u32x2 fl;
#pragma unroll
    for (int i = 53; i < 63; ++i) {
      bar();
      if (i == I_O1 + 3 && tid == 256) publish(p.ws, WS_FLAG + 128 * cu, tag);
      if (i == 60) asm volatile("buffer_load_dwordx2 %0, %1, %2, %3 offen sc1" : "=v"(fl) : "v"(128 * (lane & 3)), "s"(sC), "s"(WS_FLAG + 128 * (cu - hh)) : "memory");
      if (i < 61) kv(i); else if (i == 61) DMA3(" nt", L + L_V + vslot + 3072 * w, vV[0], vV[1], vV[2], sV, vofs);
      if (i == I_O1) {
#pragma unroll
        for (int f = 0; f < 12; ++f) st_sc1(of[f], 16 * lane, sOW, ob + h0 * 49152 + 1024 * f);
      }
      if (i == I_WO) { WO_TOUCH(ghead(1)); WO_TOUCH(ghead(2)); }
      if (i == 62) ld12_sc1(of, 16 * lane, sOW, ob + ghead(1) * 49152);
      const int n = (i < 61 ? 6 : i == 61 ? 3 : 12) + (i == I_O1 + 3 || i == 60 ? 1 : 0) + (i == I_O1 || i == I_O1 + 1 ? 12 : 0) + (i == I_WO || i == I_WO + 1 ? 4 : 0);
      switch (n - (i == I_O1 + 3 ? 1 : 0)) {
        case 3: waitvm<3>(); break; case 6: waitvm<6>(); break; case 7: waitvm<7>(); break; case 10: waitvm<10>(); break; case 11: waitvm<11>(); break; case 12: waitvm<12>(); break;
        case 16: waitvm<16>(); break; case 18: waitvm<18>(); break; case 19: waitvm<19>(); break; case 22: waitvm<22>(); break; default: waitvm<0>(); }
      if (i == 61) {
        PIN(fl);
        if (!__all((((u64)fl[1] << 32) | fl[0]) == tag)) await4(p.ws, WS_FLAG + 128 * (cu - hh), 128, tag, lane);
      }
    }
    bar();
    WO_TILE(3); WO_TILE(4);
    ST(3);
    bar();
    WO_TILE(5);
    WO_TOUCH(ghead(3)); WO_TOUCH(ghead(4)); WO_TOUCH(ghead(5)); WO_TOUCH(ghead(6));
    WO_TILE(6); WO_TILE(7);
    ST(4);
    wait_pin12<18>(of);
    ST(5);
    bar();
    oproj<3, 3, true>(acc, wb, of);
    PIN(jk);
    ST(6);
    ld12_sc1(of, 16 * lane, sOW, ob + ghead(3) * 49152); WO_TILE(11); WO_TILE(12); WO_TILE(13);
    wait_pin12<10>(of);
    ST(7);
    bar();
    oproj<9, 3, true>(acc, wb, of);
    ST(9);
    ld12_sc1(of, 16 * lane, sOW, ob + ghead(5) * 49152); WO_TILE(17); WO_TILE(18); WO_TILE(19);
    wait_pin12<10>(of);
    ST(10);
    bar();
    oproj<15, 3, true>(acc, wb, of);
    ST(11);
#pragma unroll
    for (int i = 0; i < 20; ++i) lds_wrf(L + 20480 + 20480 * w + 1024 * i + 16 * lane, f32x4{acc[i >> 2][4 * (i & 3)], acc[i >> 2][4 * (i & 3) + 1], acc[i >> 2][4 * (i & 3) + 2], acc[i >> 2][4 * (i & 3) + 3]});
    ST(12);
    bar();
  }

  const unsigned lane = lane_id();
  {
    const unsigned r32 = lane & 31, hi = lane >> 5;
    const srd_t sPW = mk_srd((char*)p.ws + WS_P, P_BYTES);
    const unsigned pgrp = grp * 4 + hh;
    bar();
    ST(13);
    if (tid == 0) publish(p.ws, WS_FLAG + 128 * (512 + cu), tag);
    await4(p.ws, WS_FLAG + 128 * (512 + cu - 4 * hg), 512, tag, lane);
    ST(14);
    if (tid == 0 && hg == 0 && hh == 0) publish(p.ws, WS_EPOCH + 256 * grp, tag);
    const srd_t sOut = mk_srd(p.out, OUT_BYTES);
    for (unsigned u = wv; u < 10; u += 8) {
      bf16x8 pr[3];
      const unsigned so = pgrp * 4 * 40960 + 10240 * hg + 1024 * u;
      asm volatile("buffer_load_dwordx4 %0, %3, %4, %5 offen sc1\n\tbuffer_load_dwordx4 %1, %3, %4, %6 offen sc1\n\t"
                   "buffer_load_dwordx4 %2, %3, %4, %7 offen sc1\n\ts_waitcnt vmcnt(0)"
                   : "=&v"(pr[0]), "=&v"(pr[1]), "=&v"(pr[2])
                   : "v"(16 * lane), "s"(sPW), "s"(so + 40960 * ((hg + 1) & 3)), "s"(so + 40960 * ((hg + 2) & 3)), "s"(so + 40960 * ((hg + 3) & 3)) : "memory");
      const f32x4 m0 = lds_rdf(L + 122880 + 2048 * u + 16 * lane), m1 = lds_rdf(L + 122880 + 2048 * u + 1024 + 16 * lane);
      float sum[8];
#pragma unroll
      for (int e = 0; e < 8; ++e) sum[e] = ((e < 4 ? m0[e & 3] : m1[e & 3]) + (float)pr[0][e]) + ((float)pr[1][e] + (float)pr[2][e]);
      const u32x4 res = {cvtpk(sum[0], sum[1]), cvtpk(sum[2], sum[3]), cvtpk(sum[4], sum[5]), cvtpk(sum[6], sum[7])};
      __builtin_amdgcn_raw_buffer_store_b128(res, sOut, r32 * 2560 + 16 * hi, (b * 128 + 32 * hg) * 2560 + br * 1280 + hh * 320 + 32 * u, 0);
    }
  }
}

extern "C" void kernel_launch(void* const* d_in, const int* in_sizes, int n_in, void* d_out, int out_size, void* d_ws, size_t ws_size, hipStream_t stream) {
  Params p;
  for (int i = 0; i < 2; ++i) { p.x[i] = d_in[5 * i]; p.k[i] = d_in[5 * i + 1]; p.v[i] = d_in[5 * i + 2]; p.wq[i] = d_in[5 * i + 3]; p.wo[i] = d_in[5 * i + 4]; }
  p.out = d_out; p.ws = d_ws;
  (void)hipFuncSetAttribute((const void*)mha2_fused, hipFuncAttributeMaxDynamicSharedMemorySize, (int)LDS_BYTES);
  hipLaunchKernelGGL(mha2_fused, dim3(256), dim3(512), LDS_BYTES, stream, p);
}
```

```cpp
#define IO1 59
#define HROT 3
#define PMIN 60
#define PACE2 1410
#define PACE 1600
#include <hip/hip_runtime.h>
#include <hip/hip_bf16.h>

#define DI __device__ __forceinline__
typedef __bf16 bf16x8 __attribute__((ext_vector_type(8)));
typedef __bf16 bf16x2 __attribute__((ext_vector_type(2)));
typedef short s16x4 __attribute__((ext_vector_type(4)));
typedef short s16x8 __attribute__((ext_vector_type(8)));
typedef float f32x2 __attribute__((ext_vector_type(2)));
typedef float f32x4 __attribute__((ext_vector_type(4)));
typedef unsigned u32x2 __attribute__((ext_vector_type(2)));
typedef unsigned u32x4 __attribute__((ext_vector_type(4)));
typedef unsigned long long u64;
typedef __amdgpu_buffer_rsrc_t srd_t;
#define LAS __attribute__((address_space(3)))
#define GAS __attribute__((address_space(1)))

constexpr unsigned ITEM = 1024u * 192u * 2u;
constexpr unsigned KV_BYTES = 256u * ITEM, X_BYTES = 8u * 128u * 640u * 2u, W_BYTES = 640u * 6144u * 2u, OUT_BYTES = 1024u * 1280u * 2u;
constexpr unsigned WS_EPOCH = 0, WS_FLAG = 4096, CTL_BYTES = 4096u + 3u * 1024u * 128u;
constexpr unsigned WS_O = 1u << 20, O_BYTES = 512u * 49152u, WS_P = WS_O + O_BYTES, P_BYTES = 256u * 40960u;
constexpr unsigned WS_STAMP = WS_P + P_BYTES;
constexpr unsigned L_K = 0, L_V = 36864, L_G = 73728, LDS_BYTES = 163840;
constexpr unsigned OWN_BUF = 143360;
constexpr unsigned park(int f) { return 16384u * (f >> 2) + 1024u * (f & 3); }
constexpr float CS = 0.07216878364870322f * 1.4426950408889634f;
constexpr float THR = 6.0f;
constexpr unsigned SPIN_CAP = 1u << 18;

struct Params { const void *x[2], *k[2], *v[2], *wq[2], *wo[2]; void* out; void* ws; };

DI srd_t mk_srd(const void* p, unsigned bytes) { return __builtin_amdgcn_make_buffer_rsrc((void*)p, (short)0, (int)bytes, 0x00020000); }
DI unsigned cvtpk(float lo, float hi) { f32x2 v = {lo, hi}; return __builtin_bit_cast(unsigned, __builtin_convertvector(v, bf16x2)); }
DI bf16x8 lds_rd(unsigned a) { return *(const LAS bf16x8*)(size_t)a; }
DI void lds_wr(unsigned a, bf16x8 v) { *(LAS bf16x8*)(size_t)a = v; }
DI f32x4 lds_rdf(unsigned a) { return *(const LAS f32x4*)(size_t)a; }
DI void lds_wrf(unsigned a, f32x4 v) { *(LAS f32x4*)(size_t)a = v; }
template <int N> DI void waitvm() { asm volatile("s_waitcnt vmcnt(%0)" ::"i"(N) : "memory"); }
DI void bar() { __builtin_amdgcn_s_waitcnt(0xc07f); asm volatile("s_barrier" ::: "memory"); }
#define SBAR() __builtin_amdgcn_sched_barrier(0)
#define PIN(x) asm volatile("" : "+v"(x))
DI unsigned lane_id() { unsigned l; asm volatile("v_mbcnt_lo_u32_b32 %0, -1, 0\n\tv_mbcnt_hi_u32_b32 %0, -1, %0" : "=v"(l)); return l; }

#define DMA3(POL, m0v, v0, v1, v2, srd, so) \
  asm volatile("s_mov_b32 m0, %0\n\ts_nop 0\n\tbuffer_load_dwordx4 %1, %4, %5 offen" POL " lds\n\tbuffer_load_dwordx4 %2, %4, %5 offen offset:1024" POL " lds\n\t" \
               "buffer_load_dwordx4 %3, %4, %5 offen offset:2048" POL " lds" ::"s"(m0v), "v"(v0), "v"(v1), "v"(v2), "s"(srd), "s"(so) : "memory")
#define DMA4X(POL, m0v, v0, v1, srd, so) \
  asm volatile("s_mov_b32 m0, %0\n\ts_nop 0\n\tbuffer_load_dwordx4 %1, %3, %4 offen" POL " lds\n\tbuffer_load_dwordx4 %2, %3, %5 offen offset:1024" POL " lds\n\t" \
               "buffer_load_dwordx4 %1, %3, %6 offen offset:2048" POL " lds\n\tbuffer_load_dwordx4 %2, %3, %7 offen offset:3072" POL " lds" \
               ::"s"(m0v), "v"(v0), "v"(v1), "s"(srd), "s"(so), "s"((so) + 9216u), "s"((so) + 18432u), "s"((so) + 27648u) : "memory")
#define DMA4O(m0v, v0, srd, so) \
  asm volatile("s_mov_b32 m0, %0\n\ts_nop 0\n\tbuffer_load_dwordx4 %1, %2, %3 offen sc1 lds\n\tbuffer_load_dwordx4 %1, %2, %3 offen offset:1024 sc1 lds\n\t" \
               "buffer_load_dwordx4 %1, %2, %3 offen offset:2048 sc1 lds\n\tbuffer_load_dwordx4 %1, %2, %3 offen offset:3072 sc1 lds" ::"s"(m0v), "v"(v0), "s"(srd), "s"(so) : "memory")
#define DMA5(POL, m0v, vo, srd, so) \
  asm volatile("s_mov_b32 m0, %0\n\ts_nop 0\n\tbuffer_load_dwordx4 %2, %7, %8 offen" POL " lds\n\tbuffer_load_dwordx4 %3, %7, %8 offen offset:1024" POL " lds\n\t" \
               "buffer_load_dwordx4 %4, %7, %8 offen offset:2048" POL " lds\n\tbuffer_load_dwordx4 %5, %7, %8 offen offset:3072" POL " lds\n\t" \
               "s_mov_b32 m0, %1\n\ts_nop 0\n\tbuffer_load_dwordx4 %6, %7, %8 offen" POL " lds" \
               ::"s"(m0v), "s"((m0v) + 4096u), "v"(vo[0]), "v"(vo[1]), "v"(vo[2]), "v"(vo[3]), "v"(vo[4]), "s"(srd), "s"(so) : "memory")
DI void touch(unsigned& jk, unsigned voff, srd_t s, unsigned so) {
  asm volatile("buffer_load_dword %0, %1, %2, %3 offen" : "+v"(jk) : "v"(voff), "s"(s), "s"(so) : "memory");
}
constexpr unsigned OOB = 0x7fffff00u;
DI void st_sc1(bf16x8 v, unsigned voff, srd_t s, unsigned so) { asm volatile("buffer_store_dwordx4 %0, %1, %2, %3 offen sc1\n\ts_nop 1" ::"v"(v), "v"(voff), "s"(s), "s"(so) : "memory"); }
DI void st12_sc1(const bf16x8 (&r)[12], unsigned voff, srd_t s, unsigned so) {
#pragma unroll
  for (int f = 0; f < 12; ++f) st_sc1(r[f], voff, s, so + 1024 * f);
}
DI void ld_epoch(u32x2& e, srd_t s, unsigned so) { asm volatile("buffer_load_dwordx2 %0, off, %1, %2 sc1" : "=v"(e) : "s"(s), "s"(so) : "memory"); }
DI u64 mk_tag(u32x2 e) {
  const u64 v = ((u64)(unsigned)__builtin_amdgcn_readfirstlane(e[1]) << 32) | (unsigned)__builtin_amdgcn_readfirstlane(e[0]);
  return v * 6364136223846793005ull + 1442695040888963407ull;
}
DI void publish(void* ws, unsigned off, u64 tag) { __hip_atomic_store((GAS u64*)((char*)ws + off), tag, __ATOMIC_RELAXED, __HIP_MEMORY_SCOPE_AGENT); }
DI unsigned flag_off(unsigned kind, unsigned cu, unsigned w) { return WS_FLAG + 128 * (1024 * kind + 4 * cu + w); }
DI void await3(void* ws, unsigned off, u64 tag) {
  const GAS u64* f = (const GAS u64*)((char*)ws + off);
#pragma nounroll
  for (unsigned s = 0; s < SPIN_CAP; ++s) {
    if (__all(__hip_atomic_load(f, __ATOMIC_RELAXED, __HIP_MEMORY_SCOPE_AGENT) == tag)) break;
    __builtin_amdgcn_s_sleep(1);
  }
  asm volatile("" ::: "memory");
}

DI f32x4 mfma16(bf16x8 a, bf16x8 b, f32x4 c) { return __builtin_amdgcn_mfma_f32_16x16x32_bf16(a, b, c, 0, 0, 0); }
DI void mfma16_acc(f32x4& d, bf16x8 a, bf16x8 b) { asm volatile("v_mfma_f32_16x16x32_bf16 %0, %1, %2, %0" : "+v"(d) : "v"(a), "v"(b)); }
DI void mfma16_c(f32x4& d, bf16x8 a, bf16x8 b, f32x4 c) { asm volatile("v_mfma_f32_16x16x32_bf16 %0, %1, %2, %3" : "=&v"(d) : "v"(a), "v"(b), "v"(c)); }
DI void mfma16_0(f32x4& d, bf16x8 a, bf16x8 b) { asm volatile("v_mfma_f32_16x16x32_bf16 %0, %1, %2, 0" : "=&v"(d) : "v"(a), "v"(b)); }
DI void mfma_settle() { asm volatile("s_nop 7" ::: "memory"); SBAR(); }
DI void swap16(unsigned& x, unsigned& y) { auto r = __builtin_amdgcn_permlane16_swap(x, y, false, false); x = r[0]; y = r[1]; }
DI void swap32(unsigned& x, unsigned& y) { auto r = __builtin_amdgcn_permlane32_swap(x, y, false, false); x = r[0]; y = r[1]; }
DI float max4g(float m) {
  unsigned x = __float_as_uint(m), y = x; swap16(x, y); x = y = __float_as_uint(__builtin_fmaxf(__uint_as_float(x), __uint_as_float(y)));
  swap32(x, y); return __builtin_fmaxf(__uint_as_float(x), __uint_as_float(y));
}
DI float sum4g(float m) {
  unsigned x = __float_as_uint(m), y = x; swap16(x, y); x = y = __float_as_uint(__uint_as_float(x) + __uint_as_float(y));
  swap32(x, y); return __uint_as_float(x) + __uint_as_float(y);
}
DI bf16x8 pack_rows(f32x4 x, f32x4 y) {
  unsigned a0 = cvtpk(x[0], x[1]), a1 = cvtpk(x[2], x[3]), b0 = cvtpk(y[0], y[1]), b1 = cvtpk(y[2], y[3]);
  swap32(a0, b0); swap16(a0, b0); swap32(a1, b1); swap16(a1, b1);
  return __builtin_bit_cast(bf16x8, u32x4{a0, a1, b0, b1});
}
DI bf16x8 pack2(f32x4 x, f32x4 y) { return __builtin_bit_cast(bf16x8, u32x4{cvtpk(x[0], x[1]), cvtpk(x[2], x[3]), cvtpk(y[0], y[1]), cvtpk(y[2], y[3])}); }
DI s16x4 lds_trh(unsigned a) { return __builtin_amdgcn_ds_read_tr16_b64_v4i16((LAS s16x4*)(size_t)a); }
DI bf16x8 cat(s16x4 lo, s16x4 hi) { s16x8 r = __builtin_shufflevector(lo, hi, 0, 1, 2, 3, 4, 5, 6, 7); return __builtin_bit_cast(bf16x8, r); }

#define ST(i)
#define STR(i)
#define ST_IV(a)

struct NoHook { DI void operator()(int) const {} };
constexpr int KD = 5, NTL = 3;
struct Tile { f32x4 t[4]; };
struct AttnState { f32x4 O[24], NM[2]; bf16x8 Q[12], vf[NTL], p[2]; float l0, l1, mx; unsigned kb[2], vb[4];
};

DI bf16x8 k_rd(const AttnState& a, int j, int slot) { return lds_rd(a.kb[(j >> 1) & 1] + 128 * (j >> 2) + 6144 * (j & 1) + 12288 * slot); }
DI s16x4 v_half(const AttnState& a, int dt, int u, int slot) { return lds_trh(a.vb[dt & 3] + 128 * (dt >> 2) + 6144 * u + 12288 * slot); }
DI void qk(const AttnState& a, Tile& s) {
#pragma unroll
  for (int j = 0; j < 12; ++j) {
    const bf16x8 kf = k_rd(a, j, 0);
#pragma unroll
    for (int h = 0; h < 2; ++h) s.t[2 * (j & 1) + h] = mfma16(kf, a.Q[(j & ~1) + h], j < 2 ? f32x4{} : s.t[2 * (j & 1) + h]);
  }
}
DI float rowmax(const Tile& s, int h) {
  const f32x4 x = s.t[h], y = s.t[2 + h];
  return max4g(__builtin_fmaxf(__builtin_fmaxf(__builtin_fmaxf(x[0], x[1]), __builtin_fmaxf(x[2], x[3])), __builtin_fmaxf(__builtin_fmaxf(y[0], y[1]), __builtin_fmaxf(y[2], y[3]))));
}
template <class F = NoHook> DI void pv_tail(AttnState& a, const Tile& sp, F kreads = F()) {
#define PVT(i) mfma16_acc(a.O[24 - 2 * NTL + (i)], a.vf[(i) >> 1], a.p[(i) & 1]); SBAR()
  PVT(0); kreads(0); kreads(1);
  PVT(1); kreads(2); kreads(3);
  PVT(2); kreads(4); { asm volatile("v_add_f32_e32 %0, %0, %1" : "+v"(a.l0) : "v"(sp.t[2][0])); } SBAR();
  PVT(3); { asm volatile("v_add_f32_e32 %0, %0, %2\n\tv_add_f32_e32 %1, %1, %3" : "+v"(a.l1), "+v"(a.l0) : "v"(sp.t[3][0]), "v"(sp.t[2][1])); } SBAR();
  PVT(4); { asm volatile("v_add_f32_e32 %0, %0, %2\n\tv_add_f32_e32 %1, %1, %3" : "+v"(a.l1), "+v"(a.l0) : "v"(sp.t[3][1]), "v"(sp.t[2][2])); } SBAR();
  PVT(5); { asm volatile("v_add_f32_e32 %0, %0, %2\n\tv_add_f32_e32 %1, %1, %3\n\tv_add_f32_e32 %0, %0, %4" : "+v"(a.l1), "+v"(a.l0) : "v"(sp.t[3][2]), "v"(sp.t[2][3]), "v"(sp.t[3][3])); } SBAR();
#undef PVT
}
DI void start_item(AttnState& a, Tile& s) {
#pragma unroll
  for (int h = 0; h < 2; ++h) { const float m = rowmax(s, h); s.t[h] -= m; s.t[2 + h] -= m; a.NM[h] = f32x4{-m, -m, -m, -m}; }
  a.l0 = 0.f; a.l1 = 0.f; a.mx = 0.f;
#pragma unroll
  for (int t = 0; t < 24; ++t) { a.O[t] = f32x4{}; PIN(a.O[t]); }
  PIN(a.NM[0]); PIN(a.NM[1]);
}
template <int MODE, int SLOT> DI void attn_step(AttnState& a, Tile& sc, Tile& sn, const unsigned qo = 0) {
  constexpr bool QK = MODE != 3;
  bar();
  ST_IV(a);
  bf16x8 kf[12];
  u32x4 p0, p1;
  if (MODE == 2) {
#pragma unroll
    for (int f = 0; f < 12; ++f) a.Q[f] = lds_rd(qo + park(f));
  }
  auto kreads = [&](int i) { if (QK) kf[i] = k_rd(a, i, (SLOT + 1) % 3); SBAR(); };
  SBAR();
  if (MODE == 1) {
#pragma unroll
    for (int i = 0; i < KD; ++i) kreads(i);
  } else {
    pv_tail(a, sn, kreads);
    if (__builtin_expect(__any(a.mx > THR), 0)) {
      mfma_settle();
#pragma unroll
      for (int t = 24 - 2 * NTL; t < 24; ++t) PIN(a.O[t]);
#pragma unroll
      for (int h = 0; h < 2; ++h) {
        const float dl = __builtin_fmaxf(rowmax(sc, h), 0.f), f = __builtin_amdgcn_exp2f(-dl);
        if (h) a.l1 *= f; else a.l0 *= f;
        sc.t[h] -= dl; sc.t[2 + h] -= dl; a.NM[h] -= dl;
#pragma unroll
        for (int t = 0; t < 12; ++t) a.O[2 * t + h] *= f;
      }
    }
    SBAR();
  }
  s16x4 vh[24];
  float m0, m1;
#define QKM(i) if (QK) { if ((i) >= 4) mfma16_acc(sn.t[(i) & 3], kf[(i) >> 1], a.Q[2 * ((i) >> 2) + ((i) & 1)]); else if (MODE == 2) mfma16_0(sn.t[(i) & 3], kf[(i) >> 1], a.Q[(i) & 1]); \
                         else mfma16_c(sn.t[(i) & 3], kf[(i) >> 1], a.Q[(i) & 1], a.NM[(i) & 1]); } SBAR()
#define PVM(i) mfma16_acc(a.O[i], cat(vh[(i) & ~1], vh[(i) | 1]), __builtin_bit_cast(bf16x8, ((i) & 1) ? p1 : p0)); SBAR()
#define KR(j) if (QK) kf[j] = k_rd(a, j, (SLOT + 1) % 3); SBAR()
#define VR(j) vh[j] = v_half(a, (j) >> 1, (j) & 1, SLOT); SBAR()
#define WT(n) __builtin_amdgcn_s_waitcnt(0xc07f | ((n) << 8)); SBAR()
  QKM(0); KR(5); { float t0; asm volatile("v_exp_f32_e32 %0, %1" : "=&v"(t0) : "v"(sc.t[0][0])); sc.t[0][0] = t0; } SBAR();
  QKM(1); { float t0; asm volatile("v_exp_f32_e32 %0, %1" : "=&v"(t0) : "v"(sc.t[0][1])); sc.t[0][1] = t0; } SBAR();
  QKM(2); KR(6); { float t0; asm volatile("v_exp_f32_e32 %0, %1" : "=&v"(t0) : "v"(sc.t[0][2])); sc.t[0][2] = t0; } SBAR(); WT(3);
  QKM(3); { float t0; asm volatile("v_exp_f32_e32 %0, %1" : "=&v"(t0) : "v"(sc.t[0][3])); sc.t[0][3] = t0; } SBAR();
  QKM(4); KR(7); { float t0; asm volatile("v_exp_f32_e32 %0, %1" : "=&v"(t0) : "v"(sc.t[2][0])); sc.t[2][0] = t0; } SBAR();
  QKM(5); { float t0; asm volatile("v_exp_f32_e32 %0, %1" : "=&v"(t0) : "v"(sc.t[2][1])); sc.t[2][1] = t0; } SBAR();
  QKM(6); KR(8); { float t0; asm volatile("v_exp_f32_e32 %0, %1" : "=&v"(t0) : "v"(sc.t[2][2])); sc.t[2][2] = t0; } SBAR(); WT(3);
  QKM(7); { float t0; asm volatile("v_exp_f32_e32 %0, %1" : "=&v"(t0) : "v"(sc.t[2][3])); sc.t[2][3] = t0; } SBAR();
  QKM(8); KR(9); { float t0; asm volatile("v_exp_f32_e32 %0, %1" : "=&v"(t0) : "v"(sc.t[1][0])); sc.t[1][0] = t0; } SBAR();
  QKM(9); { float t0; asm volatile("v_exp_f32_e32 %0, %1" : "=&v"(t0) : "v"(sc.t[1][1])); sc.t[1][1] = t0; } SBAR();
  QKM(10); KR(10); { float t0; asm volatile("v_exp_f32_e32 %0, %1" : "=&v"(t0) : "v"(sc.t[1][2])); sc.t[1][2] = t0; } SBAR(); WT(3);
  QKM(11); { float t0; asm volatile("v_exp_f32_e32 %0, %1" : "=&v"(t0) : "v"(sc.t[1][3])); sc.t[1][3] = t0; } SBAR();
  QKM(12); KR(11); { float t0; asm volatile("v_exp_f32_e32 %0, %1" : "=&v"(t0) : "v"(sc.t[3][0])); sc.t[3][0] = t0; } SBAR();
  QKM(13); { float t0; asm volatile("v_exp_f32_e32 %0, %1" : "=&v"(t0) : "v"(sc.t[3][1])); sc.t[3][1] = t0; } SBAR();
  QKM(14); { float t0; asm volatile("v_exp_f32_e32 %0, %1" : "=&v"(t0) : "v"(sc.t[3][2])); sc.t[3][2] = t0; } SBAR();
  QKM(15); { float t0; asm volatile("v_exp_f32_e32 %0, %1" : "=&v"(t0) : "v"(sc.t[3][3])); sc.t[3][3] = t0; } SBAR();
  QKM(16); VR(0); { unsigned t0; asm volatile("v_cvt_pk_bf16_f32 %0, %1, %2" : "=&v"(t0) : "v"(sc.t[0][0]), "v"(sc.t[0][1])); p0[0] = t0; } SBAR();
  QKM(17); VR(1); { unsigned t0; asm volatile("v_cvt_pk_bf16_f32 %0, %1, %2" : "=&v"(t0) : "v"(sc.t[0][2]), "v"(sc.t[0][3])); p0[1] = t0; } SBAR();
  QKM(18); VR(2); { unsigned t0; asm volatile("v_cvt_pk_bf16_f32 %0, %1, %2" : "=&v"(t0) : "v"(sc.t[2][0]), "v"(sc.t[2][1])); p0[2] = t0; } SBAR();
  QKM(19); VR(3); { unsigned t0; asm volatile("v_cvt_pk_bf16_f32 %0, %1, %2" : "=&v"(t0) : "v"(sc.t[2][2]), "v"(sc.t[2][3])); p0[3] = t0; } SBAR();
  QKM(20); VR(4); { unsigned t0; asm volatile("v_cvt_pk_bf16_f32 %0, %1, %2" : "=&v"(t0) : "v"(sc.t[1][0]), "v"(sc.t[1][1])); p1[0] = t0; } SBAR();
  QKM(21); VR(5); { unsigned t0; asm volatile("v_cvt_pk_bf16_f32 %0, %1, %2" : "=&v"(t0) : "v"(sc.t[1][2]), "v"(sc.t[1][3])); p1[1] = t0; } SBAR();
  QKM(22); VR(6); { unsigned t0; asm volatile("v_cvt_pk_bf16_f32 %0, %1, %2" : "=&v"(t0) : "v"(sc.t[3][0]), "v"(sc.t[3][1])); p1[2] = t0; } SBAR();
  QKM(23); VR(7); { unsigned t0; asm volatile("v_cvt_pk_bf16_f32 %0, %1, %2" : "=&v"(t0) : "v"(sc.t[3][2]), "v"(sc.t[3][3])); p1[3] = t0; } SBAR();
  PVM(0); VR(8);
  PVM(1); VR(9); { asm volatile("v_add_f32_e32 %0, %0, %1" : "+v"(a.l0) : "v"(sc.t[0][0])); } SBAR();
  PVM(2); VR(10); { asm volatile("v_add_f32_e32 %0, %0, %1" : "+v"(a.l1) : "v"(sc.t[1][0])); } SBAR();
  PVM(3); VR(11); { asm volatile("v_add_f32_e32 %0, %0, %1" : "+v"(a.l0) : "v"(sc.t[0][1])); } SBAR();
  PVM(4); VR(12); if (QK) { asm volatile("v_max3_f32 %0, %1, %2, %3" : "=&v"(m0) : "v"(sn.t[0][0]), "v"(sn.t[0][1]), "v"(sn.t[0][2])); } SBAR();
  PVM(5); VR(13); if (QK) { asm volatile("v_max3_f32 %0, %1, %2, %3" : "=&v"(m1) : "v"(sn.t[0][3]), "v"(sn.t[1][0]), "v"(sn.t[1][1])); } SBAR();
  PVM(6); VR(14); if (QK) { asm volatile("v_max3_f32 %0, %0, %1, %2" : "+v"(m0) : "v"(sn.t[1][2]), "v"(sn.t[1][3])); } SBAR();
  PVM(7); VR(15); if (QK) { asm volatile("v_max3_f32 %0, %0, %1, %2" : "+v"(m1) : "v"(sn.t[2][0]), "v"(sn.t[2][1])); } SBAR();
  PVM(8); VR(16); if (QK) { asm volatile("v_max3_f32 %0, %0, %1, %2" : "+v"(m0) : "v"(sn.t[2][2]), "v"(sn.t[2][3])); } SBAR();
  PVM(9); VR(17); if (QK) { asm volatile("v_max3_f32 %0, %0, %1, %2" : "+v"(m1) : "v"(sn.t[3][0]), "v"(sn.t[3][1])); } SBAR();
  PVM(10); VR(18); if (QK) { asm volatile("v_max3_f32 %0, %0, %1, %2" : "+v"(m0) : "v"(sn.t[3][2]), "v"(sn.t[3][3])); } SBAR();
  PVM(11); VR(19); if (QK) { asm volatile("v_max_f32_e32 %0, %0, %1" : "+v"(m0) : "v"(m1)); } SBAR();
  PVM(12); VR(20); { asm volatile("v_add_f32_e32 %0, %0, %1" : "+v"(a.l1) : "v"(sc.t[1][1])); } SBAR();
  PVM(13); VR(21); { asm volatile("v_add_f32_e32 %0, %0, %1" : "+v"(a.l0) : "v"(sc.t[0][2])); } SBAR();
  PVM(14); VR(22); VR(23);
  PVM(15); { asm volatile("v_add_f32_e32 %0, %0, %1" : "+v"(a.l1) : "v"(sc.t[1][2])); } SBAR();
  PVM(16); { asm volatile("v_add_f32_e32 %0, %0, %2\n\tv_add_f32_e32 %1, %1, %3" : "+v"(a.l0), "+v"(a.l1) : "v"(sc.t[0][3]), "v"(sc.t[1][3])); } SBAR();
  PVM(17);
  if (QK) a.mx = m0;
#undef QKM
#undef PVM
#undef KR
#undef VR
#undef WT
#pragma unroll
  for (int i = 0; i < NTL; ++i) a.vf[i] = cat(vh[2 * (12 - NTL + i)], vh[2 * (12 - NTL + i) + 1]);
  a.p[0] = __builtin_bit_cast(bf16x8, p0); a.p[1] = __builtin_bit_cast(bf16x8, p1);
}
template <int R, class F = NoHook> DI void attn_item(AttnState& a, Tile& sA, Tile& sB, const unsigned qo, F hook = F()) {
  constexpr int S0 = 2 * R;
  start_item(a, sA);
  attn_step<1, S0>(a, sA, sB);
#pragma nounroll
  for (int k = 0; k < 5; ++k) {
    attn_step<0, (S0 + 1) % 3>(a, sB, sA); attn_step<0, (S0 + 2) % 3>(a, sA, sB); attn_step<0, S0>(a, sB, sA);
    attn_step<0, (S0 + 1) % 3>(a, sA, sB); attn_step<0, (S0 + 2) % 3>(a, sB, sA); attn_step<0, S0>(a, sA, sB);
    hook(k);
  }
  attn_step<2 + R, (S0 + 1) % 3>(a, sB, sA, qo);
}
DI void finish_o(AttnState& a, bf16x8 (&of)[12]) {
  mfma_settle();
#pragma unroll
  for (int t = 24 - 2 * NTL; t < 24; ++t) PIN(a.O[t]);
  const float i0 = __builtin_amdgcn_rcpf(sum4g(a.l0)), i1 = __builtin_amdgcn_rcpf(sum4g(a.l1));
#pragma unroll
  for (int f = 0; f < 12; ++f) { const float iv = (f & 1) ? i1 : i0; of[f] = pack2(a.O[2 * (f & ~1) + (f & 1)] * iv, a.O[2 * (f & ~1) + 2 + (f & 1)] * iv); SBAR(); }
}
DI unsigned wq_base(unsigned lane, unsigned e) { const unsigned g = lane >> 4, q4 = (lane & 15) >> 2; return 384 * (8 * g + q4) + 32 * (e ^ ((q4 >> 1) + 2 * (g & 1))) + 8 * (lane & 3); }
DI unsigned wo_base(unsigned lane, unsigned e) { const unsigned g = lane >> 4; return 320 * (4 * g + ((lane & 15) >> 2)) + 32 * (e ^ (g & 1)) + 8 * (lane & 3); }
DI unsigned x_base(unsigned lane, unsigned w, unsigned k) { return 128 * (32 * w + (lane & 15)) + 16 * ((4 * k + (lane >> 4)) ^ ((lane & 15) >> 1)); }
DI void wo_src(unsigned (&v)[5], unsigned lane) {
#pragma unroll
  for (int q = 0; q < 5; ++q) { const unsigned pp = 64 * q + lane, r = pp / 20; v[q] = r * 1280 + 16 * ((pp % 20) ^ ((r & 4) >> 1)) - (q < 4 ? 1024 * q : 0); }
}

__global__ __launch_bounds__(512, 2) void mha2_fused(Params p) {
  extern __shared__ __attribute__((aligned(16))) char lds_[];
  ST(0);
  const unsigned tid = threadIdx.x;
  const unsigned wv = __builtin_amdgcn_readfirstlane(tid >> 6), w = wv & 3, half = wv >> 2;
  const unsigned L = (unsigned)(size_t)lds_;
  const unsigned blk = blockIdx.x, xcd = blk & 7, j = blk >> 3, br = xcd >> 2, hg = xcd & 3, b = j >> 2, hh = j & 3;
  const unsigned h0 = 4 * hg + hh;
  const unsigned grp = br * 8 + b, cu = (grp * 4 + hg) * 4 + hh;
  const unsigned kv0 = (b * 32 + h0) * ITEM;
  const unsigned ob = grp * 32 * 49152 + 12288 * w;
  auto ghead = [&](int g) { return g == 0 ? h0 + 16 : (g > 3 ? 16 : 0) + 4 * hg + ((hh + 1 + (g - 1) % 3) & 3); };
  const srd_t sOW = mk_srd((char*)p.ws + WS_O, O_BYTES), sC = mk_srd(p.ws, CTL_BYTES);
  u64 tag;

  if (half == 0) {
    const unsigned lane = tid & 63;
    const unsigned q4 = (lane & 15) >> 2, p4 = lane & 3, bq = q4 >> 1;
    AttnState a;
    u32x2 ep;
    const unsigned qo = L + L_G + 4096 * w + 16 * lane;
    {
      ld_epoch(ep, sC, WS_EPOCH + 256 * grp);
      unsigned xb[2], wb[4];
#pragma unroll
      for (int k = 0; k < 2; ++k) xb[k] = L + 81920 + x_base(lane, w, k);
#pragma unroll
      for (int e = 0; e < 4; ++e) wb[e] = L + 81920 + 16384 + wq_base(lane, e);
      f32x4 qa[24];
      s16x4 wh[12];
      bf16x8 xf[4];
#pragma unroll
      for (int t = 0; t < 24; ++t) { qa[t] = f32x4{}; PIN(qa[t]); }
#pragma unroll
      for (int t = 6; t < 12; ++t) { wh[t] = s16x4{}; PIN(wh[t]); }
      xf[2] = bf16x8{}; xf[3] = bf16x8{}; PIN(xf[2]); PIN(xf[3]);
#define QM(i) mfma16_acc(qa[(i) % 24], cat(wh[((i) & ~1) % 12], wh[((i) | 1) % 12]), xf[2 * ((i) / 24) + ((i) & 1)]); SBAR()
#define WR(h) wh[(h) % 12] = lds_trh(wb[((h) >> 1) & 3] + 128 * ((((h) >> 1) % 12) >> 2) + 12288 * ((h) / 24) + 1536 * ((h) & 1)); SBAR()
#define XR(k) xf[k] = lds_rd(xb[(k) >> 1] + 2048 * ((k) & 1)); SBAR()
      unsigned buf = 2;
#pragma nounroll
      for (int g = 0; g < 10; ++g) {
        bar(); STR(20 + g);
        SBAR();
        QM(42); XR(0); WR(0); QM(43); WR(1); XR(1);
        QM(44); WR(2); WR(3); QM(45); WR(4); WR(5);
        QM(46); WR(6); WR(7); QM(47); WR(8); WR(9);
        QM(0); WR(10); QM(1); WR(11);
        QM(2); WR(12); QM(3); WR(13);
        QM(4); WR(14); QM(5); WR(15);
        QM(6); WR(16); QM(7); WR(17);
        QM(8); XR(2); WR(18); QM(9); WR(19);
        QM(10); XR(3); WR(20); QM(11); WR(21);
        QM(12); WR(22); QM(13); WR(23);
        QM(14); WR(24); QM(15); WR(25);
        QM(16); WR(26); QM(17); WR(27);
        QM(18); WR(28); QM(19); WR(29);
        QM(20); WR(30); QM(21); WR(31);
        QM(22); WR(32); QM(23); WR(33);
        QM(24); WR(34); QM(25); WR(35);
        QM(26); WR(36); QM(27); WR(37);
        QM(28); WR(38); QM(29); WR(39);
        QM(30); WR(40); QM(31); WR(41);
        QM(32); WR(42); QM(33); WR(43);
        QM(34); WR(44); QM(35); WR(45);
        QM(36); WR(46); QM(37); WR(47);
        QM(38); QM(39);
        QM(40); QM(41);
        const int d = buf == 3 ? -122880 : 40960;
        xb[0] += d; xb[1] += d;
#pragma unroll
        for (int e = 0; e < 4; ++e) wb[e] += d;
        buf = (buf + 1) & 3;
      }
      bar();
      QM(42); QM(43); QM(44); QM(45); QM(46); QM(47);
#undef QM
#undef WR
#undef XR
      mfma_settle();
#pragma unroll
      for (int t = 18; t < 24; ++t) PIN(qa[t]);
#pragma unroll
      for (int f = 0; f < 12; ++f) a.Q[f] = pack_rows(qa[2 * (f & ~1) + (f & 1)] * CS, qa[2 * (f & ~1) + 2 + (f & 1)] * CS);
      asm volatile("s_waitcnt vmcnt(0)" : "+v"(ep)::"memory");
      tag = mk_tag(ep);
    }
    ST(1);
#pragma unroll
    for (int k = 0; k < 2; ++k) a.kb[k] = L + L_K + 384 * (lane & 15) + 16 * ((4 * k + (lane >> 4)) ^ ((lane & 15) >> 1));
#pragma unroll
    for (int e = 0; e < 4; ++e) a.vb[e] = L + L_V + 384 * (4 * (lane >> 4) + q4) + 32 * (e ^ ((2 * (lane >> 4) + bq) & 3)) + 8 * p4;
    Tile sA, sB;
    qk(a, sA);
    attn_item<0>(a, sA, sB, qo);
    {
      pv_tail(a, sB);
      bf16x8 o1[12];
      finish_o(a, o1);
#pragma unroll
      for (int f = 0; f < 12; ++f) lds_wr(qo + park(f), o1[f]);
    }
    ST(2);
    attn_item<1>(a, sA, sB, 0);
    ST(3);
    const unsigned ln = lane_id();
    bf16x8 of[12];
    f32x4 acc[20];
#define WO_AT(head, kk, at) DMA5("", L + (at) + 5120 * w, vO, sO, ((head) * 192 + 64 * (kk) + 16 * w) * 1280 + hh * 320)
#define O_AT(head, kk, at) DMA4O(L + (at) + 4096 * w, 16 * lane, sOW, ob + (head) * 49152 + 4096 * (kk))
    const srd_t sPW = mk_srd((char*)p.ws + WS_P, P_BYTES);
#define FIN(m) do { if (w == hg) { _Pragma("unroll") for (int g = 0; g < 4; ++g) lds_wrf(L + OWN_BUF + 4096 * (m) + 2048 * (g & 1) + 1024 * (g >> 1) + 16 * ln, acc[4 * (m) + g]); } \
    else { _Pragma("unroll") for (int h = 0; h < 2; ++h) st_sc1(pack2(acc[4 * (m) + h], acc[4 * (m) + 2 + h]), 16 * ln, sPW, ((grp * 4 + hh) * 4 + hg) * 40960 + 10240 * w + 2048 * (m) + 1024 * h); } SBAR(); } while (0)
#define FINISH_O2() do { pv_tail(a, sB); finish_o(a, of); } while (0)
    bar();
    FINISH_O2();
    ST(4);
    unsigned wlo0 = L + wo_base(ln, 0), wlo1 = L + wo_base(ln, 1), whi0 = wlo0 + 122880, whi1 = wlo1 + 122880, wx0 = wlo0 + 49152, wx1 = wlo1 + 49152;
    unsigned olo = L + 40960 + 4096 * w + 16 * ln, ohi = olo + 49152;
    PIN(wlo0); PIN(wlo1); PIN(whi0); PIN(whi1); PIN(wx0); PIN(wx1); PIN(olo); PIN(ohi);
    s16x4 wh[12]; bf16x8 xf[8];
    _Pragma("unroll") for (int t = 0; t < 20; ++t) { acc[t] = f32x4{}; PIN(acc[t]); }
    xf[0] = lds_rd(olo + 32768); xf[1] = lds_rd(olo + 33792); wh[0] = lds_trh(whi0 + 0); wh[1] = lds_trh(whi0 + 5120); wh[2] = lds_trh(whi1 + 0); wh[3] = lds_trh(whi1 + 5120); wh[4] = lds_trh(whi0 + 64); wh[5] = lds_trh(whi0 + 5184); wh[6] = lds_trh(whi1 + 64); wh[7] = lds_trh(whi1 + 5184); SBAR();
    mfma16_acc(acc[0], cat(wh[0], wh[1]), xf[0]); SBAR(); wh[8] = lds_trh(whi0 + 128); wh[9] = lds_trh(whi0 + 5248); SBAR();
    mfma16_acc(acc[1], cat(wh[0], wh[1]), xf[1]); SBAR(); wh[10] = lds_trh(whi1 + 128); wh[11] = lds_trh(whi1 + 5248); SBAR();
    mfma16_acc(acc[2], cat(wh[2], wh[3]), xf[0]); SBAR(); wh[0] = lds_trh(whi0 + 192); SBAR();
    mfma16_acc(acc[3], cat(wh[2], wh[3]), xf[1]); SBAR(); wh[1] = lds_trh(whi0 + 5312); SBAR();
    mfma16_acc(acc[4], cat(wh[4], wh[5]), xf[0]); SBAR(); wh[2] = lds_trh(whi1 + 192); SBAR();
    mfma16_acc(acc[5], cat(wh[4], wh[5]), xf[1]); SBAR(); wh[3] = lds_trh(whi1 + 5312); SBAR(); st_sc1(of[0], 16 * ln, sOW, ob + (h0 + 16) * 49152 + 0); SBAR();
    mfma16_acc(acc[6], cat(wh[6], wh[7]), xf[0]); SBAR(); xf[2] = lds_rd(olo + 34816); wh[4] = lds_trh(whi0 + 256); SBAR();
    mfma16_acc(acc[7], cat(wh[6], wh[7]), xf[1]); SBAR(); wh[5] = lds_trh(whi0 + 5376); SBAR();
    mfma16_acc(acc[8], cat(wh[8], wh[9]), xf[0]); SBAR(); xf[3] = lds_rd(olo + 35840); wh[6] = lds_trh(whi1 + 256); SBAR();
    mfma16_acc(acc[9], cat(wh[8], wh[9]), xf[1]); SBAR(); wh[7] = lds_trh(whi1 + 5376); SBAR();
    mfma16_acc(acc[10], cat(wh[10], wh[11]), xf[0]); SBAR(); wh[8] = lds_trh(whi0 + 10240); SBAR();
    mfma16_acc(acc[11], cat(wh[10], wh[11]), xf[1]); SBAR(); wh[9] = lds_trh(whi0 + 15360); SBAR();
    mfma16_acc(acc[12], cat(wh[0], wh[1]), xf[0]); SBAR(); wh[10] = lds_trh(whi1 + 10240); SBAR();
    mfma16_acc(acc[13], cat(wh[0], wh[1]), xf[1]); SBAR(); wh[11] = lds_trh(whi1 + 15360); SBAR();
    mfma16_acc(acc[14], cat(wh[2], wh[3]), xf[0]); SBAR(); wh[0] = lds_trh(whi0 + 10304); SBAR();
    mfma16_acc(acc[15], cat(wh[2], wh[3]), xf[1]); SBAR(); wh[1] = lds_trh(whi0 + 15424); SBAR();
    mfma16_acc(acc[16], cat(wh[4], wh[5]), xf[0]); SBAR(); wh[2] = lds_trh(whi1 + 10304); SBAR();
    mfma16_acc(acc[17], cat(wh[4], wh[5]), xf[1]); SBAR(); wh[3] = lds_trh(whi1 + 15424); SBAR();
    mfma16_acc(acc[18], cat(wh[6], wh[7]), xf[0]); SBAR(); wh[4] = lds_trh(whi0 + 10368); SBAR();
    mfma16_acc(acc[19], cat(wh[6], wh[7]), xf[1]); SBAR(); wh[5] = lds_trh(whi0 + 15488); SBAR(); st_sc1(of[1], 16 * ln, sOW, ob + (h0 + 16) * 49152 + 1024); SBAR();
    mfma16_acc(acc[0], cat(wh[8], wh[9]), xf[2]); SBAR(); wh[6] = lds_trh(whi1 + 10368); SBAR();
    mfma16_acc(acc[1], cat(wh[8], wh[9]), xf[3]); SBAR(); wh[7] = lds_trh(whi1 + 15488); SBAR();
    mfma16_acc(acc[2], cat(wh[10], wh[11]), xf[2]); SBAR(); wh[8] = lds_trh(whi0 + 10432); SBAR();
    mfma16_acc(acc[3], cat(wh[10], wh[11]), xf[3]); SBAR(); wh[9] = lds_trh(whi0 + 15552); SBAR();
    mfma16_acc(acc[4], cat(wh[0], wh[1]), xf[2]); SBAR(); wh[10] = lds_trh(whi1 + 10432); SBAR();
    mfma16_acc(acc[5], cat(wh[0], wh[1]), xf[3]); SBAR(); wh[11] = lds_trh(whi1 + 15552); SBAR();
    mfma16_acc(acc[6], cat(wh[2], wh[3]), xf[2]); SBAR(); wh[0] = lds_trh(whi0 + 10496); SBAR();
    mfma16_acc(acc[7], cat(wh[2], wh[3]), xf[3]); SBAR(); wh[1] = lds_trh(whi0 + 15616); SBAR();
    mfma16_acc(acc[8], cat(wh[4], wh[5]), xf[2]); SBAR(); wh[2] = lds_trh(whi1 + 10496); SBAR();
    mfma16_acc(acc[9], cat(wh[4], wh[5]), xf[3]); SBAR(); wh[3] = lds_trh(whi1 + 15616); SBAR();
    mfma16_acc(acc[10], cat(wh[6], wh[7]), xf[2]); SBAR();
    mfma16_acc(acc[11], cat(wh[6], wh[7]), xf[3]); SBAR();
    mfma16_acc(acc[12], cat(wh[8], wh[9]), xf[2]); SBAR();
    mfma16_acc(acc[13], cat(wh[8], wh[9]), xf[3]); SBAR(); st_sc1(of[2], 16 * ln, sOW, ob + (h0 + 16) * 49152 + 2048); SBAR();
    bar();
    SBAR();
    mfma16_acc(acc[14], cat(wh[10], wh[11]), xf[2]); SBAR(); xf[4] = lds_rd(ohi + 0); wh[4] = lds_trh(whi0 + 20480); SBAR();
    mfma16_acc(acc[15], cat(wh[10], wh[11]), xf[3]); SBAR(); xf[5] = lds_rd(ohi + 1024); wh[5] = lds_trh(whi0 + 25600); SBAR();
    mfma16_acc(acc[16], cat(wh[0], wh[1]), xf[2]); SBAR(); wh[6] = lds_trh(whi1 + 20480); wh[7] = lds_trh(whi1 + 25600); SBAR();
    mfma16_acc(acc[17], cat(wh[0], wh[1]), xf[3]); SBAR(); wh[8] = lds_trh(whi0 + 20544); wh[9] = lds_trh(whi0 + 25664); SBAR();
    mfma16_acc(acc[18], cat(wh[2], wh[3]), xf[2]); SBAR(); wh[10] = lds_trh(whi1 + 20544); wh[11] = lds_trh(whi1 + 25664); SBAR();
    mfma16_acc(acc[19], cat(wh[2], wh[3]), xf[3]); SBAR(); wh[0] = lds_trh(whi0 + 20608); wh[1] = lds_trh(whi0 + 25728); SBAR();
    mfma16_acc(acc[0], cat(wh[4], wh[5]), xf[4]); SBAR(); wh[2] = lds_trh(whi1 + 20608); SBAR();
    mfma16_acc(acc[1], cat(wh[4], wh[5]), xf[5]); SBAR(); wh[3] = lds_trh(whi1 + 25728); SBAR();
    mfma16_acc(acc[2], cat(wh[6], wh[7]), xf[4]); SBAR(); wh[4] = lds_trh(whi0 + 20672); SBAR();
    mfma16_acc(acc[3], cat(wh[6], wh[7]), xf[5]); SBAR(); wh[5] = lds_trh(whi0 + 25792); SBAR();
    mfma16_acc(acc[4], cat(wh[8], wh[9]), xf[4]); SBAR(); wh[6] = lds_trh(whi1 + 20672); SBAR();
    mfma16_acc(acc[5], cat(wh[8], wh[9]), xf[5]); SBAR(); wh[7] = lds_trh(whi1 + 25792); SBAR();
    mfma16_acc(acc[6], cat(wh[10], wh[11]), xf[4]); SBAR(); xf[6] = lds_rd(ohi + 2048); wh[8] = lds_trh(whi0 + 20736); SBAR();
    mfma16_acc(acc[7], cat(wh[10], wh[11]), xf[5]); SBAR(); wh[9] = lds_trh(whi0 + 25856); SBAR(); st_sc1(of[3], 16 * ln, sOW, ob + (h0 + 16) * 49152 + 3072); SBAR();
    mfma16_acc(acc[8], cat(wh[0], wh[1]), xf[4]); SBAR(); xf[7] = lds_rd(ohi + 3072); wh[10] = lds_trh(whi1 + 20736); SBAR();
    mfma16_acc(acc[9], cat(wh[0], wh[1]), xf[5]); SBAR(); wh[11] = lds_trh(whi1 + 25856); SBAR();
    mfma16_acc(acc[10], cat(wh[2], wh[3]), xf[4]); SBAR(); wh[0] = lds_trh(whi0 + 30720); SBAR();
    mfma16_acc(acc[11], cat(wh[2], wh[3]), xf[5]); SBAR(); wh[1] = lds_trh(whi0 + 35840); SBAR();
    mfma16_acc(acc[12], cat(wh[4], wh[5]), xf[4]); SBAR(); wh[2] = lds_trh(whi1 + 30720); SBAR();
    mfma16_acc(acc[13], cat(wh[4], wh[5]), xf[5]); SBAR(); wh[3] = lds_trh(whi1 + 35840); SBAR();
    mfma16_acc(acc[14], cat(wh[6], wh[7]), xf[4]); SBAR(); wh[4] = lds_trh(whi0 + 30784); SBAR();
    mfma16_acc(acc[15], cat(wh[6], wh[7]), xf[5]); SBAR(); wh[5] = lds_trh(whi0 + 35904); SBAR();
    mfma16_acc(acc[16], cat(wh[8], wh[9]), xf[4]); SBAR(); wh[6] = lds_trh(whi1 + 30784); SBAR();
    mfma16_acc(acc[17], cat(wh[8], wh[9]), xf[5]); SBAR(); wh[7] = lds_trh(whi1 + 35904); SBAR();
    mfma16_acc(acc[18], cat(wh[10], wh[11]), xf[4]); SBAR(); wh[8] = lds_trh(whi0 + 30848); SBAR();
    mfma16_acc(acc[19], cat(wh[10], wh[11]), xf[5]); SBAR(); wh[9] = lds_trh(whi0 + 35968); SBAR();
    mfma16_acc(acc[0], cat(wh[0], wh[1]), xf[6]); SBAR(); wh[10] = lds_trh(whi1 + 30848); SBAR();
    mfma16_acc(acc[1], cat(wh[0], wh[1]), xf[7]); SBAR(); wh[11] = lds_trh(whi1 + 35968); SBAR(); st_sc1(of[4], 16 * ln, sOW, ob + (h0 + 16) * 49152 + 4096); SBAR();
    mfma16_acc(acc[2], cat(wh[2], wh[3]), xf[6]); SBAR(); wh[0] = lds_trh(whi0 + 30912); SBAR();
    mfma16_acc(acc[3], cat(wh[2], wh[3]), xf[7]); SBAR(); wh[1] = lds_trh(whi0 + 36032); SBAR();
    mfma16_acc(acc[4], cat(wh[4], wh[5]), xf[6]); SBAR(); wh[2] = lds_trh(whi1 + 30912); SBAR();
    mfma16_acc(acc[5], cat(wh[4], wh[5]), xf[7]); SBAR(); wh[3] = lds_trh(whi1 + 36032); SBAR();
    mfma16_acc(acc[6], cat(wh[6], wh[7]), xf[6]); SBAR(); wh[4] = lds_trh(whi0 + 30976); SBAR();
    mfma16_acc(acc[7], cat(wh[6], wh[7]), xf[7]); SBAR(); wh[5] = lds_trh(whi0 + 36096); SBAR();
    mfma16_acc(acc[8], cat(wh[8], wh[9]), xf[6]); SBAR(); wh[6] = lds_trh(whi1 + 30976); SBAR();
    mfma16_acc(acc[9], cat(wh[8], wh[9]), xf[7]); SBAR(); wh[7] = lds_trh(whi1 + 36096); SBAR();
    mfma16_acc(acc[10], cat(wh[10], wh[11]), xf[6]); SBAR();
    mfma16_acc(acc[11], cat(wh[10], wh[11]), xf[7]); SBAR();
    mfma16_acc(acc[12], cat(wh[0], wh[1]), xf[6]); SBAR();
    mfma16_acc(acc[13], cat(wh[0], wh[1]), xf[7]); SBAR();
    bar();
    SBAR();
    mfma16_acc(acc[14], cat(wh[2], wh[3]), xf[6]); SBAR(); xf[0] = lds_rd(ohi + 16384); wh[8] = lds_trh(wlo0 + 0); SBAR();
    mfma16_acc(acc[15], cat(wh[2], wh[3]), xf[7]); SBAR(); xf[1] = lds_rd(ohi + 17408); wh[9] = lds_trh(wlo0 + 5120); SBAR(); st_sc1(of[5], 16 * ln, sOW, ob + (h0 + 16) * 49152 + 5120); SBAR();
    mfma16_acc(acc[16], cat(wh[4], wh[5]), xf[6]); SBAR(); wh[10] = lds_trh(wlo1 + 0); wh[11] = lds_trh(wlo1 + 5120); SBAR();
    mfma16_acc(acc[17], cat(wh[4], wh[5]), xf[7]); SBAR(); wh[0] = lds_trh(wlo0 + 64); wh[1] = lds_trh(wlo0 + 5184); SBAR();
    mfma16_acc(acc[18], cat(wh[6], wh[7]), xf[6]); SBAR(); wh[2] = lds_trh(wlo1 + 64); wh[3] = lds_trh(wlo1 + 5184); SBAR();
    mfma16_acc(acc[19], cat(wh[6], wh[7]), xf[7]); SBAR(); wh[4] = lds_trh(wlo0 + 128); wh[5] = lds_trh(wlo0 + 5248); SBAR();
    mfma16_acc(acc[0], cat(wh[8], wh[9]), xf[0]); SBAR(); wh[6] = lds_trh(wlo1 + 128); SBAR();
    mfma16_acc(acc[1], cat(wh[8], wh[9]), xf[1]); SBAR(); wh[7] = lds_trh(wlo1 + 5248); SBAR();
    mfma16_acc(acc[2], cat(wh[10], wh[11]), xf[0]); SBAR(); wh[8] = lds_trh(wlo0 + 192); SBAR();
    mfma16_acc(acc[3], cat(wh[10], wh[11]), xf[1]); SBAR(); wh[9] = lds_trh(wlo0 + 5312); SBAR();
    mfma16_acc(acc[4], cat(wh[0], wh[1]), xf[0]); SBAR(); wh[10] = lds_trh(wlo1 + 192); SBAR();
    mfma16_acc(acc[5], cat(wh[0], wh[1]), xf[1]); SBAR(); wh[11] = lds_trh(wlo1 + 5312); SBAR();
    mfma16_acc(acc[6], cat(wh[2], wh[3]), xf[0]); SBAR(); xf[2] = lds_rd(ohi + 18432); wh[0] = lds_trh(wlo0 + 256); SBAR();
    mfma16_acc(acc[7], cat(wh[2], wh[3]), xf[1]); SBAR(); wh[1] = lds_trh(wlo0 + 5376); SBAR();
    mfma16_acc(acc[8], cat(wh[4], wh[5]), xf[0]); SBAR(); xf[3] = lds_rd(ohi + 19456); wh[2] = lds_trh(wlo1 + 256); SBAR();
    mfma16_acc(acc[9], cat(wh[4], wh[5]), xf[1]); SBAR(); wh[3] = lds_trh(wlo1 + 5376); SBAR(); st_sc1(of[6], 16 * ln, sOW, ob + (h0 + 16) * 49152 + 6144); SBAR();
    mfma16_acc(acc[10], cat(wh[6], wh[7]), xf[0]); SBAR(); wh[4] = lds_trh(wlo0 + 10240); SBAR();
    mfma16_acc(acc[11], cat(wh[6], wh[7]), xf[1]); SBAR(); wh[5] = lds_trh(wlo0 + 15360); SBAR();
    mfma16_acc(acc[12], cat(wh[8], wh[9]), xf[0]); SBAR(); wh[6] = lds_trh(wlo1 + 10240); SBAR();
    mfma16_acc(acc[13], cat(wh[8], wh[9]), xf[1]); SBAR(); wh[7] = lds_trh(wlo1 + 15360); SBAR();
    mfma16_acc(acc[14], cat(wh[10], wh[11]), xf[0]); SBAR(); wh[8] = lds_trh(wlo0 + 10304); SBAR();
    mfma16_acc(acc[15], cat(wh[10], wh[11]), xf[1]); SBAR(); wh[9] = lds_trh(wlo0 + 15424); SBAR();
    mfma16_acc(acc[16], cat(wh[0], wh[1]), xf[0]); SBAR(); wh[10] = lds_trh(wlo1 + 10304); SBAR();
    mfma16_acc(acc[17], cat(wh[0], wh[1]), xf[1]); SBAR(); wh[11] = lds_trh(wlo1 + 15424); SBAR();
    mfma16_acc(acc[18], cat(wh[2], wh[3]), xf[0]); SBAR(); wh[0] = lds_trh(wlo0 + 10368); SBAR();
    mfma16_acc(acc[19], cat(wh[2], wh[3]), xf[1]); SBAR(); wh[1] = lds_trh(wlo0 + 15488); SBAR();
    mfma16_acc(acc[0], cat(wh[4], wh[5]), xf[2]); SBAR(); wh[2] = lds_trh(wlo1 + 10368); SBAR();
    mfma16_acc(acc[1], cat(wh[4], wh[5]), xf[3]); SBAR(); wh[3] = lds_trh(wlo1 + 15488); SBAR();
    mfma16_acc(acc[2], cat(wh[6], wh[7]), xf[2]); SBAR(); wh[4] = lds_trh(wlo0 + 10432); SBAR();
    mfma16_acc(acc[3], cat(wh[6], wh[7]), xf[3]); SBAR(); wh[5] = lds_trh(wlo0 + 15552); SBAR(); st_sc1(of[7], 16 * ln, sOW, ob + (h0 + 16) * 49152 + 7168); SBAR();
    mfma16_acc(acc[4], cat(wh[8], wh[9]), xf[2]); SBAR(); wh[6] = lds_trh(wlo1 + 10432); SBAR();
    mfma16_acc(acc[5], cat(wh[8], wh[9]), xf[3]); SBAR(); wh[7] = lds_trh(wlo1 + 15552); SBAR();
    mfma16_acc(acc[6], cat(wh[10], wh[11]), xf[2]); SBAR(); wh[8] = lds_trh(wlo0 + 10496); SBAR();
    mfma16_acc(acc[7], cat(wh[10], wh[11]), xf[3]); SBAR(); wh[9] = lds_trh(wlo0 + 15616); SBAR();
    mfma16_acc(acc[8], cat(wh[0], wh[1]), xf[2]); SBAR(); wh[10] = lds_trh(wlo1 + 10496); SBAR();
    mfma16_acc(acc[9], cat(wh[0], wh[1]), xf[3]); SBAR(); wh[11] = lds_trh(wlo1 + 15616); SBAR();
    mfma16_acc(acc[10], cat(wh[2], wh[3]), xf[2]); SBAR();
    mfma16_acc(acc[11], cat(wh[2], wh[3]), xf[3]); SBAR();
    mfma16_acc(acc[12], cat(wh[4], wh[5]), xf[2]); SBAR();
    mfma16_acc(acc[13], cat(wh[4], wh[5]), xf[3]); SBAR();
    bar();
    SBAR();
    ST(5);
    mfma16_acc(acc[14], cat(wh[6], wh[7]), xf[2]); SBAR(); wh[0] = lds_trh(wlo0 + 20480); wh[1] = lds_trh(wlo0 + 25600); SBAR();
    mfma16_acc(acc[15], cat(wh[6], wh[7]), xf[3]); SBAR(); wh[2] = lds_trh(wlo1 + 20480); wh[3] = lds_trh(wlo1 + 25600); SBAR();
    mfma16_acc(acc[16], cat(wh[8], wh[9]), xf[2]); SBAR(); wh[4] = lds_trh(wlo0 + 20544); wh[5] = lds_trh(wlo0 + 25664); SBAR();
    mfma16_acc(acc[17], cat(wh[8], wh[9]), xf[3]); SBAR(); wh[6] = lds_trh(wlo1 + 20544); wh[7] = lds_trh(wlo1 + 25664); SBAR(); st_sc1(of[8], 16 * ln, sOW, ob + (h0 + 16) * 49152 + 8192); SBAR();
    mfma16_acc(acc[18], cat(wh[10], wh[11]), xf[2]); SBAR(); wh[8] = lds_trh(wlo0 + 20608); SBAR();
    mfma16_acc(acc[19], cat(wh[10], wh[11]), xf[3]); SBAR(); wh[9] = lds_trh(wlo0 + 25728); SBAR();
    mfma16_acc(acc[0], cat(wh[0], wh[1]), of[0]); SBAR(); wh[10] = lds_trh(wlo1 + 20608); SBAR();
    mfma16_acc(acc[1], cat(wh[0], wh[1]), of[1]); SBAR(); wh[11] = lds_trh(wlo1 + 25728); SBAR();
    mfma16_acc(acc[2], cat(wh[2], wh[3]), of[0]); SBAR(); wh[0] = lds_trh(wlo0 + 20672); SBAR();
    mfma16_acc(acc[3], cat(wh[2], wh[3]), of[1]); SBAR(); wh[1] = lds_trh(wlo0 + 25792); SBAR();
    mfma16_acc(acc[4], cat(wh[4], wh[5]), of[0]); SBAR(); wh[2] = lds_trh(wlo1 + 20672); SBAR();
    mfma16_acc(acc[5], cat(wh[4], wh[5]), of[1]); SBAR(); wh[3] = lds_trh(wlo1 + 25792); SBAR();
    mfma16_acc(acc[6], cat(wh[6], wh[7]), of[0]); SBAR(); wh[4] = lds_trh(wlo0 + 20736); SBAR();
    mfma16_acc(acc[7], cat(wh[6], wh[7]), of[1]); SBAR(); wh[5] = lds_trh(wlo0 + 25856); SBAR();
    mfma16_acc(acc[8], cat(wh[8], wh[9]), of[0]); SBAR(); wh[6] = lds_trh(wlo1 + 20736); SBAR();
    mfma16_acc(acc[9], cat(wh[8], wh[9]), of[1]); SBAR(); wh[7] = lds_trh(wlo1 + 25856); SBAR();
    mfma16_acc(acc[10], cat(wh[10], wh[11]), of[0]); SBAR(); wh[8] = lds_trh(wlo0 + 30720); SBAR();
    mfma16_acc(acc[11], cat(wh[10], wh[11]), of[1]); SBAR(); wh[9] = lds_trh(wlo0 + 35840); SBAR(); st_sc1(of[9], 16 * ln, sOW, ob + (h0 + 16) * 49152 + 9216); SBAR();
    mfma16_acc(acc[12], cat(wh[0], wh[1]), of[0]); SBAR(); wh[10] = lds_trh(wlo1 + 30720); SBAR();
    mfma16_acc(acc[13], cat(wh[0], wh[1]), of[1]); SBAR(); wh[11] = lds_trh(wlo1 + 35840); SBAR();
    mfma16_acc(acc[14], cat(wh[2], wh[3]), of[0]); SBAR(); wh[0] = lds_trh(wlo0 + 30784); SBAR();
    mfma16_acc(acc[15], cat(wh[2], wh[3]), of[1]); SBAR(); wh[1] = lds_trh(wlo0 + 35904); SBAR();
    mfma16_acc(acc[16], cat(wh[4], wh[5]), of[0]); SBAR(); wh[2] = lds_trh(wlo1 + 30784); SBAR();
    mfma16_acc(acc[17], cat(wh[4], wh[5]), of[1]); SBAR(); wh[3] = lds_trh(wlo1 + 35904); SBAR();
    mfma16_acc(acc[18], cat(wh[6], wh[7]), of[0]); SBAR(); wh[4] = lds_trh(wlo0 + 30848); SBAR();
    mfma16_acc(acc[19], cat(wh[6], wh[7]), of[1]); SBAR(); wh[5] = lds_trh(wlo0 + 35968); SBAR();
    mfma16_acc(acc[0], cat(wh[8], wh[9]), of[2]); SBAR(); wh[6] = lds_trh(wlo1 + 30848); SBAR();
    mfma16_acc(acc[1], cat(wh[8], wh[9]), of[3]); SBAR(); wh[7] = lds_trh(wlo1 + 35968); SBAR();
    mfma16_acc(acc[2], cat(wh[10], wh[11]), of[2]); SBAR(); wh[8] = lds_trh(wlo0 + 30912); SBAR();
    mfma16_acc(acc[3], cat(wh[10], wh[11]), of[3]); SBAR(); wh[9] = lds_trh(wlo0 + 36032); SBAR();
    mfma16_acc(acc[4], cat(wh[0], wh[1]), of[2]); SBAR(); wh[10] = lds_trh(wlo1 + 30912); SBAR();
    mfma16_acc(acc[5], cat(wh[0], wh[1]), of[3]); SBAR(); wh[11] = lds_trh(wlo1 + 36032); SBAR(); st_sc1(of[10], 16 * ln, sOW, ob + (h0 + 16) * 49152 + 10240); SBAR();
    mfma16_acc(acc[6], cat(wh[2], wh[3]), of[2]); SBAR(); wh[0] = lds_trh(wlo0 + 30976); SBAR();
    mfma16_acc(acc[7], cat(wh[2], wh[3]), of[3]); SBAR(); wh[1] = lds_trh(wlo0 + 36096); SBAR();
    mfma16_acc(acc[8], cat(wh[4], wh[5]), of[2]); SBAR(); wh[2] = lds_trh(wlo1 + 30976); SBAR();
    mfma16_acc(acc[9], cat(wh[4], wh[5]), of[3]); SBAR(); wh[3] = lds_trh(wlo1 + 36096); SBAR();
    mfma16_acc(acc[10], cat(wh[6], wh[7]), of[2]); SBAR();
    mfma16_acc(acc[11], cat(wh[6], wh[7]), of[3]); SBAR();
    mfma16_acc(acc[12], cat(wh[8], wh[9]), of[2]); SBAR();
    mfma16_acc(acc[13], cat(wh[8], wh[9]), of[3]); SBAR();
    bar();
    SBAR();
    mfma16_acc(acc[14], cat(wh[10], wh[11]), of[2]); SBAR(); wh[4] = lds_trh(wx0 + 0); wh[5] = lds_trh(wx0 + 5120); SBAR();
    mfma16_acc(acc[15], cat(wh[10], wh[11]), of[3]); SBAR(); wh[6] = lds_trh(wx1 + 0); wh[7] = lds_trh(wx1 + 5120); SBAR();
    mfma16_acc(acc[16], cat(wh[0], wh[1]), of[2]); SBAR(); wh[8] = lds_trh(wx0 + 64); wh[9] = lds_trh(wx0 + 5184); SBAR();
    mfma16_acc(acc[17], cat(wh[0], wh[1]), of[3]); SBAR(); wh[10] = lds_trh(wx1 + 64); wh[11] = lds_trh(wx1 + 5184); SBAR();
    mfma16_acc(acc[18], cat(wh[2], wh[3]), of[2]); SBAR(); wh[0] = lds_trh(wx0 + 128); SBAR();
    mfma16_acc(acc[19], cat(wh[2], wh[3]), of[3]); SBAR(); wh[1] = lds_trh(wx0 + 5248); SBAR(); st_sc1(of[11], 16 * ln, sOW, ob + (h0 + 16) * 49152 + 11264); SBAR();
    mfma16_acc(acc[0], cat(wh[4], wh[5]), of[4]); SBAR(); wh[2] = lds_trh(wx1 + 128); SBAR();
    mfma16_acc(acc[1], cat(wh[4], wh[5]), of[5]); SBAR(); wh[3] = lds_trh(wx1 + 5248); SBAR();
    mfma16_acc(acc[2], cat(wh[6], wh[7]), of[4]); SBAR(); wh[4] = lds_trh(wx0 + 192); SBAR();
    mfma16_acc(acc[3], cat(wh[6], wh[7]), of[5]); SBAR(); wh[5] = lds_trh(wx0 + 5312); SBAR();
    mfma16_acc(acc[4], cat(wh[8], wh[9]), of[4]); SBAR(); wh[6] = lds_trh(wx1 + 192); SBAR();
    mfma16_acc(acc[5], cat(wh[8], wh[9]), of[5]); SBAR(); wh[7] = lds_trh(wx1 + 5312); SBAR();
    mfma16_acc(acc[6], cat(wh[10], wh[11]), of[4]); SBAR(); wh[8] = lds_trh(wx0 + 256); SBAR();
    mfma16_acc(acc[7], cat(wh[10], wh[11]), of[5]); SBAR(); wh[9] = lds_trh(wx0 + 5376); SBAR();
    mfma16_acc(acc[8], cat(wh[0], wh[1]), of[4]); SBAR(); wh[10] = lds_trh(wx1 + 256); SBAR();
    mfma16_acc(acc[9], cat(wh[0], wh[1]), of[5]); SBAR(); wh[11] = lds_trh(wx1 + 5376); SBAR();
    mfma16_acc(acc[10], cat(wh[2], wh[3]), of[4]); SBAR(); wh[0] = lds_trh(wx0 + 10240); SBAR();
    mfma16_acc(acc[11], cat(wh[2], wh[3]), of[5]); SBAR(); wh[1] = lds_trh(wx0 + 15360); SBAR();
    mfma16_acc(acc[12], cat(wh[4], wh[5]), of[4]); SBAR(); wh[2] = lds_trh(wx1 + 10240); SBAR();
    mfma16_acc(acc[13], cat(wh[4], wh[5]), of[5]); SBAR(); wh[3] = lds_trh(wx1 + 15360); SBAR();
    mfma16_acc(acc[14], cat(wh[6], wh[7]), of[4]); SBAR(); wh[4] = lds_trh(wx0 + 10304); SBAR();
    mfma16_acc(acc[15], cat(wh[6], wh[7]), of[5]); SBAR(); wh[5] = lds_trh(wx0 + 15424); SBAR();
    mfma16_acc(acc[16], cat(wh[8], wh[9]), of[4]); SBAR(); wh[6] = lds_trh(wx1 + 10304); SBAR();
    mfma16_acc(acc[17], cat(wh[8], wh[9]), of[5]); SBAR(); wh[7] = lds_trh(wx1 + 15424); SBAR();
    mfma16_acc(acc[18], cat(wh[10], wh[11]), of[4]); SBAR(); wh[8] = lds_trh(wx0 + 10368); SBAR();
    mfma16_acc(acc[19], cat(wh[10], wh[11]), of[5]); SBAR(); wh[9] = lds_trh(wx0 + 15488); SBAR();
    mfma16_acc(acc[0], cat(wh[0], wh[1]), of[6]); SBAR(); wh[10] = lds_trh(wx1 + 10368); SBAR();
    mfma16_acc(acc[1], cat(wh[0], wh[1]), of[7]); SBAR(); wh[11] = lds_trh(wx1 + 15488); SBAR();
    mfma16_acc(acc[2], cat(wh[2], wh[3]), of[6]); SBAR(); wh[0] = lds_trh(wx0 + 10432); SBAR();
    mfma16_acc(acc[3], cat(wh[2], wh[3]), of[7]); SBAR(); wh[1] = lds_trh(wx0 + 15552); SBAR();
    mfma16_acc(acc[4], cat(wh[4], wh[5]), of[6]); SBAR(); wh[2] = lds_trh(wx1 + 10432); SBAR();
    mfma16_acc(acc[5], cat(wh[4], wh[5]), of[7]); SBAR(); wh[3] = lds_trh(wx1 + 15552); SBAR();
    mfma16_acc(acc[6], cat(wh[6], wh[7]), of[6]); SBAR(); wh[4] = lds_trh(wx0 + 10496); SBAR();
    mfma16_acc(acc[7], cat(wh[6], wh[7]), of[7]); SBAR(); wh[5] = lds_trh(wx0 + 15616); SBAR();
    mfma16_acc(acc[8], cat(wh[8], wh[9]), of[6]); SBAR(); wh[6] = lds_trh(wx1 + 10496); SBAR();
    mfma16_acc(acc[9], cat(wh[8], wh[9]), of[7]); SBAR(); wh[7] = lds_trh(wx1 + 15616); SBAR();
    mfma16_acc(acc[10], cat(wh[10], wh[11]), of[6]); SBAR();
    mfma16_acc(acc[11], cat(wh[10], wh[11]), of[7]); SBAR();
    mfma16_acc(acc[12], cat(wh[0], wh[1]), of[6]); SBAR();
    mfma16_acc(acc[13], cat(wh[0], wh[1]), of[7]); SBAR();
    bar();
    SBAR();
    mfma16_acc(acc[14], cat(wh[2], wh[3]), of[6]); SBAR(); wh[8] = lds_trh(whi0 + 0); wh[9] = lds_trh(whi0 + 5120); SBAR();
    mfma16_acc(acc[15], cat(wh[2], wh[3]), of[7]); SBAR(); wh[10] = lds_trh(whi1 + 0); wh[11] = lds_trh(whi1 + 5120); SBAR();
    mfma16_acc(acc[16], cat(wh[4], wh[5]), of[6]); SBAR(); wh[0] = lds_trh(whi0 + 64); wh[1] = lds_trh(whi0 + 5184); SBAR();
    mfma16_acc(acc[17], cat(wh[4], wh[5]), of[7]); SBAR(); wh[2] = lds_trh(whi1 + 64); wh[3] = lds_trh(whi1 + 5184); SBAR();
    mfma16_acc(acc[18], cat(wh[6], wh[7]), of[6]); SBAR(); wh[4] = lds_trh(whi0 + 128); SBAR();
    mfma16_acc(acc[19], cat(wh[6], wh[7]), of[7]); SBAR(); wh[5] = lds_trh(whi0 + 5248); SBAR();
    mfma16_acc(acc[0], cat(wh[8], wh[9]), of[8]); SBAR(); wh[6] = lds_trh(whi1 + 128); SBAR();
    mfma16_acc(acc[1], cat(wh[8], wh[9]), of[9]); SBAR(); wh[7] = lds_trh(whi1 + 5248); SBAR();
    mfma16_acc(acc[2], cat(wh[10], wh[11]), of[8]); SBAR(); wh[8] = lds_trh(whi0 + 192); SBAR();
    mfma16_acc(acc[3], cat(wh[10], wh[11]), of[9]); SBAR(); wh[9] = lds_trh(whi0 + 5312); SBAR();
    mfma16_acc(acc[4], cat(wh[0], wh[1]), of[8]); SBAR(); wh[10] = lds_trh(whi1 + 192); SBAR();
    mfma16_acc(acc[5], cat(wh[0], wh[1]), of[9]); SBAR(); wh[11] = lds_trh(whi1 + 5312); SBAR();
    mfma16_acc(acc[6], cat(wh[2], wh[3]), of[8]); SBAR(); wh[0] = lds_trh(whi0 + 256); SBAR();
    mfma16_acc(acc[7], cat(wh[2], wh[3]), of[9]); SBAR(); wh[1] = lds_trh(whi0 + 5376); SBAR();
    mfma16_acc(acc[8], cat(wh[4], wh[5]), of[8]); SBAR(); wh[2] = lds_trh(whi1 + 256); SBAR();
    mfma16_acc(acc[9], cat(wh[4], wh[5]), of[9]); SBAR(); wh[3] = lds_trh(whi1 + 5376); SBAR();
    mfma16_acc(acc[10], cat(wh[6], wh[7]), of[8]); SBAR(); wh[4] = lds_trh(whi0 + 10240); SBAR();
    mfma16_acc(acc[11], cat(wh[6], wh[7]), of[9]); SBAR(); wh[5] = lds_trh(whi0 + 15360); SBAR();
    mfma16_acc(acc[12], cat(wh[8], wh[9]), of[8]); SBAR(); wh[6] = lds_trh(whi1 + 10240); SBAR();
    mfma16_acc(acc[13], cat(wh[8], wh[9]), of[9]); SBAR(); wh[7] = lds_trh(whi1 + 15360); SBAR();
    mfma16_acc(acc[14], cat(wh[10], wh[11]), of[8]); SBAR(); wh[8] = lds_trh(whi0 + 10304); SBAR();
    mfma16_acc(acc[15], cat(wh[10], wh[11]), of[9]); SBAR(); wh[9] = lds_trh(whi0 + 15424); SBAR();
    mfma16_acc(acc[16], cat(wh[0], wh[1]), of[8]); SBAR(); wh[10] = lds_trh(whi1 + 10304); SBAR();
    mfma16_acc(acc[17], cat(wh[0], wh[1]), of[9]); SBAR(); wh[11] = lds_trh(whi1 + 15424); SBAR();
    mfma16_acc(acc[18], cat(wh[2], wh[3]), of[8]); SBAR(); wh[0] = lds_trh(whi0 + 10368); SBAR();
    mfma16_acc(acc[19], cat(wh[2], wh[3]), of[9]); SBAR(); wh[1] = lds_trh(whi0 + 15488); SBAR();
    mfma16_acc(acc[0], cat(wh[4], wh[5]), of[10]); SBAR(); wh[2] = lds_trh(whi1 + 10368); SBAR();
    mfma16_acc(acc[1], cat(wh[4], wh[5]), of[11]); SBAR(); wh[3] = lds_trh(whi1 + 15488); SBAR();
    mfma16_acc(acc[2], cat(wh[6], wh[7]), of[10]); SBAR(); wh[4] = lds_trh(whi0 + 10432); SBAR();
    mfma16_acc(acc[3], cat(wh[6], wh[7]), of[11]); SBAR(); wh[5] = lds_trh(whi0 + 15552); SBAR();
    mfma16_acc(acc[4], cat(wh[8], wh[9]), of[10]); SBAR(); wh[6] = lds_trh(whi1 + 10432); SBAR();
    mfma16_acc(acc[5], cat(wh[8], wh[9]), of[11]); SBAR(); wh[7] = lds_trh(whi1 + 15552); SBAR();
    mfma16_acc(acc[6], cat(wh[10], wh[11]), of[10]); SBAR(); wh[8] = lds_trh(whi0 + 10496); SBAR();
    mfma16_acc(acc[7], cat(wh[10], wh[11]), of[11]); SBAR(); wh[9] = lds_trh(whi0 + 15616); SBAR();
    mfma16_acc(acc[8], cat(wh[0], wh[1]), of[10]); SBAR(); wh[10] = lds_trh(whi1 + 10496); SBAR();
    mfma16_acc(acc[9], cat(wh[0], wh[1]), of[11]); SBAR(); wh[11] = lds_trh(whi1 + 15616); SBAR();
    mfma16_acc(acc[10], cat(wh[2], wh[3]), of[10]); SBAR();
    mfma16_acc(acc[11], cat(wh[2], wh[3]), of[11]); SBAR();
    mfma16_acc(acc[12], cat(wh[4], wh[5]), of[10]); SBAR();
    mfma16_acc(acc[13], cat(wh[4], wh[5]), of[11]); SBAR();
    bar();
    SBAR();
    ST(6);
    mfma16_acc(acc[14], cat(wh[6], wh[7]), of[10]); SBAR(); xf[0] = lds_rd(olo + 32768); wh[0] = lds_trh(whi0 + 20480); SBAR();
    mfma16_acc(acc[15], cat(wh[6], wh[7]), of[11]); SBAR(); xf[1] = lds_rd(olo + 33792); wh[1] = lds_trh(whi0 + 25600); SBAR();
    mfma16_acc(acc[16], cat(wh[8], wh[9]), of[10]); SBAR(); wh[2] = lds_trh(whi1 + 20480); wh[3] = lds_trh(whi1 + 25600); SBAR();
    mfma16_acc(acc[17], cat(wh[8], wh[9]), of[11]); SBAR(); wh[4] = lds_trh(whi0 + 20544); wh[5] = lds_trh(whi0 + 25664); SBAR();
    mfma16_acc(acc[18], cat(wh[10], wh[11]), of[10]); SBAR(); wh[6] = lds_trh(whi1 + 20544); wh[7] = lds_trh(whi1 + 25664); SBAR();
    mfma16_acc(acc[19], cat(wh[10], wh[11]), of[11]); SBAR(); wh[8] = lds_trh(whi0 + 20608); wh[9] = lds_trh(whi0 + 25728); SBAR();
    mfma16_acc(acc[0], cat(wh[0], wh[1]), xf[0]); SBAR(); wh[10] = lds_trh(whi1 + 20608); SBAR();
    mfma16_acc(acc[1], cat(wh[0], wh[1]), xf[1]); SBAR(); wh[11] = lds_trh(whi1 + 25728); SBAR();
    mfma16_acc(acc[2], cat(wh[2], wh[3]), xf[0]); SBAR(); wh[0] = lds_trh(whi0 + 20672); SBAR();
    mfma16_acc(acc[3], cat(wh[2], wh[3]), xf[1]); SBAR(); wh[1] = lds_trh(whi0 + 25792); SBAR();
    mfma16_acc(acc[4], cat(wh[4], wh[5]), xf[0]); SBAR(); wh[2] = lds_trh(whi1 + 20672); SBAR();
    mfma16_acc(acc[5], cat(wh[4], wh[5]), xf[1]); SBAR(); wh[3] = lds_trh(whi1 + 25792); SBAR();
    mfma16_acc(acc[6], cat(wh[6], wh[7]), xf[0]); SBAR(); xf[2] = lds_rd(olo + 34816); wh[4] = lds_trh(whi0 + 20736); SBAR();
    mfma16_acc(acc[7], cat(wh[6], wh[7]), xf[1]); SBAR(); wh[5] = lds_trh(whi0 + 25856); SBAR();
    mfma16_acc(acc[8], cat(wh[8], wh[9]), xf[0]); SBAR(); xf[3] = lds_rd(olo + 35840); wh[6] = lds_trh(whi1 + 20736); SBAR();
    mfma16_acc(acc[9], cat(wh[8], wh[9]), xf[1]); SBAR(); wh[7] = lds_trh(whi1 + 25856); SBAR();
    mfma16_acc(acc[10], cat(wh[10], wh[11]), xf[0]); SBAR(); wh[8] = lds_trh(whi0 + 30720); SBAR();
    mfma16_acc(acc[11], cat(wh[10], wh[11]), xf[1]); SBAR(); wh[9] = lds_trh(whi0 + 35840); SBAR();
    mfma16_acc(acc[12], cat(wh[0], wh[1]), xf[0]); SBAR(); wh[10] = lds_trh(whi1 + 30720); SBAR();
    mfma16_acc(acc[13], cat(wh[0], wh[1]), xf[1]); SBAR(); wh[11] = lds_trh(whi1 + 35840); SBAR();
    mfma16_acc(acc[14], cat(wh[2], wh[3]), xf[0]); SBAR(); wh[0] = lds_trh(whi0 + 30784); SBAR();
    mfma16_acc(acc[15], cat(wh[2], wh[3]), xf[1]); SBAR(); wh[1] = lds_trh(whi0 + 35904); SBAR();
    mfma16_acc(acc[16], cat(wh[4], wh[5]), xf[0]); SBAR(); wh[2] = lds_trh(whi1 + 30784); SBAR();
    mfma16_acc(acc[17], cat(wh[4], wh[5]), xf[1]); SBAR(); wh[3] = lds_trh(whi1 + 35904); SBAR();
    mfma16_acc(acc[18], cat(wh[6], wh[7]), xf[0]); SBAR(); wh[4] = lds_trh(whi0 + 30848); SBAR();
    mfma16_acc(acc[19], cat(wh[6], wh[7]), xf[1]); SBAR(); wh[5] = lds_trh(whi0 + 35968); SBAR();
    mfma16_acc(acc[0], cat(wh[8], wh[9]), xf[2]); SBAR(); wh[6] = lds_trh(whi1 + 30848); SBAR();
    mfma16_acc(acc[1], cat(wh[8], wh[9]), xf[3]); SBAR(); wh[7] = lds_trh(whi1 + 35968); SBAR();
    mfma16_acc(acc[2], cat(wh[10], wh[11]), xf[2]); SBAR(); wh[8] = lds_trh(whi0 + 30912); SBAR();
    mfma16_acc(acc[3], cat(wh[10], wh[11]), xf[3]); SBAR(); wh[9] = lds_trh(whi0 + 36032); SBAR();
    mfma16_acc(acc[4], cat(wh[0], wh[1]), xf[2]); SBAR(); wh[10] = lds_trh(whi1 + 30912); SBAR();
    mfma16_acc(acc[5], cat(wh[0], wh[1]), xf[3]); SBAR(); wh[11] = lds_trh(whi1 + 36032); SBAR();
    mfma16_acc(acc[6], cat(wh[2], wh[3]), xf[2]); SBAR(); wh[0] = lds_trh(whi0 + 30976); SBAR();
    mfma16_acc(acc[7], cat(wh[2], wh[3]), xf[3]); SBAR(); wh[1] = lds_trh(whi0 + 36096); SBAR();
    mfma16_acc(acc[8], cat(wh[4], wh[5]), xf[2]); SBAR(); wh[2] = lds_trh(whi1 + 30976); SBAR();
    mfma16_acc(acc[9], cat(wh[4], wh[5]), xf[3]); SBAR(); wh[3] = lds_trh(whi1 + 36096); SBAR();
    mfma16_acc(acc[10], cat(wh[6], wh[7]), xf[2]); SBAR();
    mfma16_acc(acc[11], cat(wh[6], wh[7]), xf[3]); SBAR();
    mfma16_acc(acc[12], cat(wh[8], wh[9]), xf[2]); SBAR();
    mfma16_acc(acc[13], cat(wh[8], wh[9]), xf[3]); SBAR();
    bar();
    SBAR();
    mfma16_acc(acc[14], cat(wh[10], wh[11]), xf[2]); SBAR(); xf[4] = lds_rd(ohi + 0); wh[4] = lds_trh(wlo0 + 0); SBAR();
    mfma16_acc(acc[15], cat(wh[10], wh[11]), xf[3]); SBAR(); xf[5] = lds_rd(ohi + 1024); wh[5] = lds_trh(wlo0 + 5120); SBAR();
    mfma16_acc(acc[16], cat(wh[0], wh[1]), xf[2]); SBAR(); wh[6] = lds_trh(wlo1 + 0); wh[7] = lds_trh(wlo1 + 5120); SBAR();
    mfma16_acc(acc[17], cat(wh[0], wh[1]), xf[3]); SBAR(); wh[8] = lds_trh(wlo0 + 64); wh[9] = lds_trh(wlo0 + 5184); SBAR();
    mfma16_acc(acc[18], cat(wh[2], wh[3]), xf[2]); SBAR(); wh[10] = lds_trh(wlo1 + 64); wh[11] = lds_trh(wlo1 + 5184); SBAR();
    mfma16_acc(acc[19], cat(wh[2], wh[3]), xf[3]); SBAR(); wh[0] = lds_trh(wlo0 + 128); wh[1] = lds_trh(wlo0 + 5248); SBAR();
    mfma16_acc(acc[0], cat(wh[4], wh[5]), xf[4]); SBAR(); wh[2] = lds_trh(wlo1 + 128); SBAR();
    mfma16_acc(acc[1], cat(wh[4], wh[5]), xf[5]); SBAR(); wh[3] = lds_trh(wlo1 + 5248); SBAR();
    mfma16_acc(acc[2], cat(wh[6], wh[7]), xf[4]); SBAR(); wh[4] = lds_trh(wlo0 + 192); SBAR();
    mfma16_acc(acc[3], cat(wh[6], wh[7]), xf[5]); SBAR(); wh[5] = lds_trh(wlo0 + 5312); SBAR();
    mfma16_acc(acc[4], cat(wh[8], wh[9]), xf[4]); SBAR(); wh[6] = lds_trh(wlo1 + 192); SBAR();
    mfma16_acc(acc[5], cat(wh[8], wh[9]), xf[5]); SBAR(); wh[7] = lds_trh(wlo1 + 5312); SBAR();
    mfma16_acc(acc[6], cat(wh[10], wh[11]), xf[4]); SBAR(); xf[6] = lds_rd(ohi + 2048); wh[8] = lds_trh(wlo0 + 256); SBAR();
    mfma16_acc(acc[7], cat(wh[10], wh[11]), xf[5]); SBAR(); wh[9] = lds_trh(wlo0 + 5376); SBAR();
    mfma16_acc(acc[8], cat(wh[0], wh[1]), xf[4]); SBAR(); xf[7] = lds_rd(ohi + 3072); wh[10] = lds_trh(wlo1 + 256); SBAR();
    mfma16_acc(acc[9], cat(wh[0], wh[1]), xf[5]); SBAR(); wh[11] = lds_trh(wlo1 + 5376); SBAR();
    mfma16_acc(acc[10], cat(wh[2], wh[3]), xf[4]); SBAR(); wh[0] = lds_trh(wlo0 + 10240); SBAR();
    mfma16_acc(acc[11], cat(wh[2], wh[3]), xf[5]); SBAR(); wh[1] = lds_trh(wlo0 + 15360); SBAR();
    mfma16_acc(acc[12], cat(wh[4], wh[5]), xf[4]); SBAR(); wh[2] = lds_trh(wlo1 + 10240); SBAR();
    mfma16_acc(acc[13], cat(wh[4], wh[5]), xf[5]); SBAR(); wh[3] = lds_trh(wlo1 + 15360); SBAR();
    mfma16_acc(acc[14], cat(wh[6], wh[7]), xf[4]); SBAR(); wh[4] = lds_trh(wlo0 + 10304); SBAR();
    mfma16_acc(acc[15], cat(wh[6], wh[7]), xf[5]); SBAR(); wh[5] = lds_trh(wlo0 + 15424); SBAR();
    mfma16_acc(acc[16], cat(wh[8], wh[9]), xf[4]); SBAR(); wh[6] = lds_trh(wlo1 + 10304); SBAR();
    mfma16_acc(acc[17], cat(wh[8], wh[9]), xf[5]); SBAR(); wh[7] = lds_trh(wlo1 + 15424); SBAR(); waitvm<0>(); if (ln == 0) publish(p.ws, flag_off(1, cu, w), tag); SBAR();
    mfma16_acc(acc[18], cat(wh[10], wh[11]), xf[4]); SBAR(); wh[8] = lds_trh(wlo0 + 10368); SBAR();
    mfma16_acc(acc[19], cat(wh[10], wh[11]), xf[5]); SBAR(); wh[9] = lds_trh(wlo0 + 15488); SBAR();
    mfma16_acc(acc[0], cat(wh[0], wh[1]), xf[6]); SBAR(); wh[10] = lds_trh(wlo1 + 10368); SBAR();
    mfma16_acc(acc[1], cat(wh[0], wh[1]), xf[7]); SBAR(); wh[11] = lds_trh(wlo1 + 15488); SBAR();
    mfma16_acc(acc[2], cat(wh[2], wh[3]), xf[6]); SBAR(); wh[0] = lds_trh(wlo0 + 10432); SBAR();
    mfma16_acc(acc[3], cat(wh[2], wh[3]), xf[7]); SBAR(); wh[1] = lds_trh(wlo0 + 15552); SBAR();
    mfma16_acc(acc[4], cat(wh[4], wh[5]), xf[6]); SBAR(); wh[2] = lds_trh(wlo1 + 10432); SBAR();
    mfma16_acc(acc[5], cat(wh[4], wh[5]), xf[7]); SBAR(); wh[3] = lds_trh(wlo1 + 15552); SBAR();
    mfma16_acc(acc[6], cat(wh[6], wh[7]), xf[6]); SBAR(); wh[4] = lds_trh(wlo0 + 10496); SBAR();
    mfma16_acc(acc[7], cat(wh[6], wh[7]), xf[7]); SBAR(); wh[5] = lds_trh(wlo0 + 15616); SBAR();
    mfma16_acc(acc[8], cat(wh[8], wh[9]), xf[6]); SBAR(); wh[6] = lds_trh(wlo1 + 10496); SBAR();
    mfma16_acc(acc[9], cat(wh[8], wh[9]), xf[7]); SBAR(); wh[7] = lds_trh(wlo1 + 15616); SBAR();
    mfma16_acc(acc[10], cat(wh[10], wh[11]), xf[6]); SBAR();
    mfma16_acc(acc[11], cat(wh[10], wh[11]), xf[7]); SBAR();
    mfma16_acc(acc[12], cat(wh[0], wh[1]), xf[6]); SBAR();
    mfma16_acc(acc[13], cat(wh[0], wh[1]), xf[7]); SBAR();
    bar();
    SBAR();
    mfma16_acc(acc[14], cat(wh[2], wh[3]), xf[6]); SBAR(); xf[0] = lds_rd(ohi + 16384); wh[8] = lds_trh(wlo0 + 20480); SBAR();
    mfma16_acc(acc[15], cat(wh[2], wh[3]), xf[7]); SBAR(); xf[1] = lds_rd(ohi + 17408); wh[9] = lds_trh(wlo0 + 25600); SBAR();
    mfma16_acc(acc[16], cat(wh[4], wh[5]), xf[6]); SBAR(); wh[10] = lds_trh(wlo1 + 20480); wh[11] = lds_trh(wlo1 + 25600); SBAR();
    mfma16_acc(acc[17], cat(wh[4], wh[5]), xf[7]); SBAR(); wh[0] = lds_trh(wlo0 + 20544); wh[1] = lds_trh(wlo0 + 25664); SBAR();
    mfma16_acc(acc[18], cat(wh[6], wh[7]), xf[6]); SBAR(); wh[2] = lds_trh(wlo1 + 20544); wh[3] = lds_trh(wlo1 + 25664); SBAR();
    mfma16_acc(acc[19], cat(wh[6], wh[7]), xf[7]); SBAR(); wh[4] = lds_trh(wlo0 + 20608); wh[5] = lds_trh(wlo0 + 25728); SBAR();
    mfma16_acc(acc[0], cat(wh[8], wh[9]), xf[0]); SBAR(); wh[6] = lds_trh(wlo1 + 20608); SBAR();
    mfma16_acc(acc[1], cat(wh[8], wh[9]), xf[1]); SBAR(); wh[7] = lds_trh(wlo1 + 25728); SBAR();
    mfma16_acc(acc[2], cat(wh[10], wh[11]), xf[0]); SBAR(); wh[8] = lds_trh(wlo0 + 20672); SBAR();
    mfma16_acc(acc[3], cat(wh[10], wh[11]), xf[1]); SBAR(); wh[9] = lds_trh(wlo0 + 25792); SBAR();
    mfma16_acc(acc[4], cat(wh[0], wh[1]), xf[0]); SBAR(); wh[10] = lds_trh(wlo1 + 20672); SBAR();
    mfma16_acc(acc[5], cat(wh[0], wh[1]), xf[1]); SBAR(); wh[11] = lds_trh(wlo1 + 25792); SBAR();
    mfma16_acc(acc[6], cat(wh[2], wh[3]), xf[0]); SBAR(); xf[2] = lds_rd(ohi + 18432); wh[0] = lds_trh(wlo0 + 20736); SBAR();
    mfma16_acc(acc[7], cat(wh[2], wh[3]), xf[1]); SBAR(); wh[1] = lds_trh(wlo0 + 25856); SBAR();
    mfma16_acc(acc[8], cat(wh[4], wh[5]), xf[0]); SBAR(); xf[3] = lds_rd(ohi + 19456); wh[2] = lds_trh(wlo1 + 20736); SBAR();
    mfma16_acc(acc[9], cat(wh[4], wh[5]), xf[1]); SBAR(); wh[3] = lds_trh(wlo1 + 25856); SBAR();
    mfma16_acc(acc[10], cat(wh[6], wh[7]), xf[0]); SBAR(); wh[4] = lds_trh(wlo0 + 30720); SBAR();
    mfma16_acc(acc[11], cat(wh[6], wh[7]), xf[1]); SBAR(); wh[5] = lds_trh(wlo0 + 35840); SBAR();
    mfma16_acc(acc[12], cat(wh[8], wh[9]), xf[0]); SBAR(); wh[6] = lds_trh(wlo1 + 30720); SBAR();
    mfma16_acc(acc[13], cat(wh[8], wh[9]), xf[1]); SBAR(); wh[7] = lds_trh(wlo1 + 35840); SBAR();
    mfma16_acc(acc[14], cat(wh[10], wh[11]), xf[0]); SBAR(); wh[8] = lds_trh(wlo0 + 30784); SBAR();
    mfma16_acc(acc[15], cat(wh[10], wh[11]), xf[1]); SBAR(); wh[9] = lds_trh(wlo0 + 35904); SBAR();
    mfma16_acc(acc[16], cat(wh[0], wh[1]), xf[0]); SBAR(); wh[10] = lds_trh(wlo1 + 30784); SBAR();
    mfma16_acc(acc[17], cat(wh[0], wh[1]), xf[1]); SBAR(); wh[11] = lds_trh(wlo1 + 35904); SBAR();
    mfma16_acc(acc[18], cat(wh[2], wh[3]), xf[0]); SBAR(); wh[0] = lds_trh(wlo0 + 30848); SBAR();
    mfma16_acc(acc[19], cat(wh[2], wh[3]), xf[1]); SBAR(); wh[1] = lds_trh(wlo0 + 35968); SBAR();
    mfma16_acc(acc[0], cat(wh[4], wh[5]), xf[2]); SBAR(); wh[2] = lds_trh(wlo1 + 30848); SBAR();
    mfma16_acc(acc[1], cat(wh[4], wh[5]), xf[3]); SBAR(); wh[3] = lds_trh(wlo1 + 35968); SBAR();
    mfma16_acc(acc[2], cat(wh[6], wh[7]), xf[2]); SBAR(); wh[4] = lds_trh(wlo0 + 30912); SBAR();
    mfma16_acc(acc[3], cat(wh[6], wh[7]), xf[3]); SBAR(); wh[5] = lds_trh(wlo0 + 36032); SBAR();
    mfma16_acc(acc[4], cat(wh[8], wh[9]), xf[2]); SBAR(); wh[6] = lds_trh(wlo1 + 30912); SBAR();
    mfma16_acc(acc[5], cat(wh[8], wh[9]), xf[3]); SBAR(); wh[7] = lds_trh(wlo1 + 36032); SBAR();
    mfma16_acc(acc[6], cat(wh[10], wh[11]), xf[2]); SBAR(); wh[8] = lds_trh(wlo0 + 30976); SBAR();
    mfma16_acc(acc[7], cat(wh[10], wh[11]), xf[3]); SBAR(); wh[9] = lds_trh(wlo0 + 36096); SBAR();
    mfma16_acc(acc[8], cat(wh[0], wh[1]), xf[2]); SBAR(); wh[10] = lds_trh(wlo1 + 30976); SBAR();
    mfma16_acc(acc[9], cat(wh[0], wh[1]), xf[3]); SBAR(); wh[11] = lds_trh(wlo1 + 36096); SBAR();
    mfma16_acc(acc[10], cat(wh[2], wh[3]), xf[2]); SBAR();
    mfma16_acc(acc[11], cat(wh[2], wh[3]), xf[3]); SBAR();
    mfma16_acc(acc[12], cat(wh[4], wh[5]), xf[2]); SBAR();
    mfma16_acc(acc[13], cat(wh[4], wh[5]), xf[3]); SBAR();
    bar();
    SBAR();
    ST(7);
    mfma16_acc(acc[14], cat(wh[6], wh[7]), xf[2]); SBAR(); xf[4] = lds_rd(olo + 0); wh[0] = lds_trh(whi0 + 0); SBAR();
    mfma16_acc(acc[15], cat(wh[6], wh[7]), xf[3]); SBAR(); xf[5] = lds_rd(olo + 1024); wh[1] = lds_trh(whi0 + 5120); SBAR();
    mfma16_acc(acc[16], cat(wh[8], wh[9]), xf[2]); SBAR(); wh[2] = lds_trh(whi1 + 0); wh[3] = lds_trh(whi1 + 5120); SBAR();
    mfma16_acc(acc[17], cat(wh[8], wh[9]), xf[3]); SBAR(); wh[4] = lds_trh(whi0 + 64); wh[5] = lds_trh(whi0 + 5184); SBAR();
    mfma16_acc(acc[18], cat(wh[10], wh[11]), xf[2]); SBAR(); wh[6] = lds_trh(whi1 + 64); wh[7] = lds_trh(whi1 + 5184); SBAR();
    mfma16_acc(acc[19], cat(wh[10], wh[11]), xf[3]); SBAR(); wh[8] = lds_trh(whi0 + 128); wh[9] = lds_trh(whi0 + 5248); SBAR();
    mfma16_acc(acc[0], cat(wh[0], wh[1]), xf[4]); SBAR(); wh[10] = lds_trh(whi1 + 128); SBAR();
    mfma16_acc(acc[1], cat(wh[0], wh[1]), xf[5]); SBAR(); wh[11] = lds_trh(whi1 + 5248); SBAR();
    mfma16_acc(acc[2], cat(wh[2], wh[3]), xf[4]); SBAR(); wh[0] = lds_trh(whi0 + 192); SBAR();
    mfma16_acc(acc[3], cat(wh[2], wh[3]), xf[5]); SBAR(); wh[1] = lds_trh(whi0 + 5312); SBAR();
    mfma16_acc(acc[4], cat(wh[4], wh[5]), xf[4]); SBAR(); wh[2] = lds_trh(whi1 + 192); SBAR();
    mfma16_acc(acc[5], cat(wh[4], wh[5]), xf[5]); SBAR(); wh[3] = lds_trh(whi1 + 5312); SBAR();
    mfma16_acc(acc[6], cat(wh[6], wh[7]), xf[4]); SBAR(); xf[6] = lds_rd(olo + 2048); wh[4] = lds_trh(whi0 + 256); SBAR();
    mfma16_acc(acc[7], cat(wh[6], wh[7]), xf[5]); SBAR(); wh[5] = lds_trh(whi0 + 5376); SBAR();
    mfma16_acc(acc[8], cat(wh[8], wh[9]), xf[4]); SBAR(); xf[7] = lds_rd(olo + 3072); wh[6] = lds_trh(whi1 + 256); SBAR();
    mfma16_acc(acc[9], cat(wh[8], wh[9]), xf[5]); SBAR(); wh[7] = lds_trh(whi1 + 5376); SBAR();
    mfma16_acc(acc[10], cat(wh[10], wh[11]), xf[4]); SBAR(); wh[8] = lds_trh(whi0 + 10240); SBAR();
    mfma16_acc(acc[11], cat(wh[10], wh[11]), xf[5]); SBAR(); wh[9] = lds_trh(whi0 + 15360); SBAR();
    mfma16_acc(acc[12], cat(wh[0], wh[1]), xf[4]); SBAR(); wh[10] = lds_trh(whi1 + 10240); SBAR();
    mfma16_acc(acc[13], cat(wh[0], wh[1]), xf[5]); SBAR(); wh[11] = lds_trh(whi1 + 15360); SBAR();
    mfma16_acc(acc[14], cat(wh[2], wh[3]), xf[4]); SBAR(); wh[0] = lds_trh(whi0 + 10304); SBAR();
    mfma16_acc(acc[15], cat(wh[2], wh[3]), xf[5]); SBAR(); wh[1] = lds_trh(whi0 + 15424); SBAR();
    mfma16_acc(acc[16], cat(wh[4], wh[5]), xf[4]); SBAR(); wh[2] = lds_trh(whi1 + 10304); SBAR();
    mfma16_acc(acc[17], cat(wh[4], wh[5]), xf[5]); SBAR(); wh[3] = lds_trh(whi1 + 15424); SBAR();
    mfma16_acc(acc[18], cat(wh[6], wh[7]), xf[4]); SBAR(); wh[4] = lds_trh(whi0 + 10368); SBAR();
    mfma16_acc(acc[19], cat(wh[6], wh[7]), xf[5]); SBAR(); wh[5] = lds_trh(whi0 + 15488); SBAR();
    mfma16_acc(acc[0], cat(wh[8], wh[9]), xf[6]); SBAR(); wh[6] = lds_trh(whi1 + 10368); SBAR();
    mfma16_acc(acc[1], cat(wh[8], wh[9]), xf[7]); SBAR(); wh[7] = lds_trh(whi1 + 15488); SBAR();
    mfma16_acc(acc[2], cat(wh[10], wh[11]), xf[6]); SBAR(); wh[8] = lds_trh(whi0 + 10432); SBAR();
    mfma16_acc(acc[3], cat(wh[10], wh[11]), xf[7]); SBAR(); wh[9] = lds_trh(whi0 + 15552); SBAR();
    mfma16_acc(acc[4], cat(wh[0], wh[1]), xf[6]); SBAR(); wh[10] = lds_trh(whi1 + 10432); SBAR();
    mfma16_acc(acc[5], cat(wh[0], wh[1]), xf[7]); SBAR(); wh[11] = lds_trh(whi1 + 15552); SBAR();
    mfma16_acc(acc[6], cat(wh[2], wh[3]), xf[6]); SBAR(); wh[0] = lds_trh(whi0 + 10496); SBAR();
    mfma16_acc(acc[7], cat(wh[2], wh[3]), xf[7]); SBAR(); wh[1] = lds_trh(whi0 + 15616); SBAR();
    mfma16_acc(acc[8], cat(wh[4], wh[5]), xf[6]); SBAR(); wh[2] = lds_trh(whi1 + 10496); SBAR();
    mfma16_acc(acc[9], cat(wh[4], wh[5]), xf[7]); SBAR(); wh[3] = lds_trh(whi1 + 15616); SBAR();
    mfma16_acc(acc[10], cat(wh[6], wh[7]), xf[6]); SBAR();
    mfma16_acc(acc[11], cat(wh[6], wh[7]), xf[7]); SBAR();
    mfma16_acc(acc[12], cat(wh[8], wh[9]), xf[6]); SBAR();
    mfma16_acc(acc[13], cat(wh[8], wh[9]), xf[7]); SBAR();
    bar();
    SBAR();
    mfma16_acc(acc[14], cat(wh[10], wh[11]), xf[6]); SBAR(); xf[0] = lds_rd(olo + 16384); wh[4] = lds_trh(whi0 + 20480); SBAR();
    mfma16_acc(acc[15], cat(wh[10], wh[11]), xf[7]); SBAR(); xf[1] = lds_rd(olo + 17408); wh[5] = lds_trh(whi0 + 25600); SBAR();
    mfma16_acc(acc[16], cat(wh[0], wh[1]), xf[6]); SBAR(); wh[6] = lds_trh(whi1 + 20480); wh[7] = lds_trh(whi1 + 25600); SBAR();
    mfma16_acc(acc[17], cat(wh[0], wh[1]), xf[7]); SBAR(); wh[8] = lds_trh(whi0 + 20544); wh[9] = lds_trh(whi0 + 25664); SBAR();
    mfma16_acc(acc[18], cat(wh[2], wh[3]), xf[6]); SBAR(); wh[10] = lds_trh(whi1 + 20544); wh[11] = lds_trh(whi1 + 25664); SBAR();
    mfma16_acc(acc[19], cat(wh[2], wh[3]), xf[7]); SBAR(); wh[0] = lds_trh(whi0 + 20608); wh[1] = lds_trh(whi0 + 25728); SBAR();
    mfma16_acc(acc[0], cat(wh[4], wh[5]), xf[0]); SBAR(); wh[2] = lds_trh(whi1 + 20608); SBAR();
    mfma16_acc(acc[1], cat(wh[4], wh[5]), xf[1]); SBAR(); wh[3] = lds_trh(whi1 + 25728); SBAR();
    mfma16_acc(acc[2], cat(wh[6], wh[7]), xf[0]); SBAR(); wh[4] = lds_trh(whi0 + 20672); SBAR();
    mfma16_acc(acc[3], cat(wh[6], wh[7]), xf[1]); SBAR(); wh[5] = lds_trh(whi0 + 25792); SBAR();
    mfma16_acc(acc[4], cat(wh[8], wh[9]), xf[0]); SBAR(); wh[6] = lds_trh(whi1 + 20672); SBAR();
    mfma16_acc(acc[5], cat(wh[8], wh[9]), xf[1]); SBAR(); wh[7] = lds_trh(whi1 + 25792); SBAR();
    mfma16_acc(acc[6], cat(wh[10], wh[11]), xf[0]); SBAR(); xf[2] = lds_rd(olo + 18432); wh[8] = lds_trh(whi0 + 20736); SBAR();
    mfma16_acc(acc[7], cat(wh[10], wh[11]), xf[1]); SBAR(); wh[9] = lds_trh(whi0 + 25856); SBAR();
    mfma16_acc(acc[8], cat(wh[0], wh[1]), xf[0]); SBAR(); xf[3] = lds_rd(olo + 19456); wh[10] = lds_trh(whi1 + 20736); SBAR();
    mfma16_acc(acc[9], cat(wh[0], wh[1]), xf[1]); SBAR(); wh[11] = lds_trh(whi1 + 25856); SBAR();
    mfma16_acc(acc[10], cat(wh[2], wh[3]), xf[0]); SBAR(); wh[0] = lds_trh(whi0 + 30720); SBAR();
    mfma16_acc(acc[11], cat(wh[2], wh[3]), xf[1]); SBAR(); wh[1] = lds_trh(whi0 + 35840); SBAR();
    mfma16_acc(acc[12], cat(wh[4], wh[5]), xf[0]); SBAR(); wh[2] = lds_trh(whi1 + 30720); SBAR();
    mfma16_acc(acc[13], cat(wh[4], wh[5]), xf[1]); SBAR(); wh[3] = lds_trh(whi1 + 35840); SBAR();
    mfma16_acc(acc[14], cat(wh[6], wh[7]), xf[0]); SBAR(); wh[4] = lds_trh(whi0 + 30784); SBAR();
    mfma16_acc(acc[15], cat(wh[6], wh[7]), xf[1]); SBAR(); wh[5] = lds_trh(whi0 + 35904); SBAR();
    mfma16_acc(acc[16], cat(wh[8], wh[9]), xf[0]); SBAR(); wh[6] = lds_trh(whi1 + 30784); SBAR();
    mfma16_acc(acc[17], cat(wh[8], wh[9]), xf[1]); SBAR(); wh[7] = lds_trh(whi1 + 35904); SBAR();
    mfma16_acc(acc[18], cat(wh[10], wh[11]), xf[0]); SBAR(); wh[8] = lds_trh(whi0 + 30848); SBAR();
    mfma16_acc(acc[19], cat(wh[10], wh[11]), xf[1]); SBAR(); wh[9] = lds_trh(whi0 + 35968); SBAR();
    mfma16_acc(acc[0], cat(wh[0], wh[1]), xf[2]); SBAR(); wh[10] = lds_trh(whi1 + 30848); SBAR();
    mfma16_acc(acc[1], cat(wh[0], wh[1]), xf[3]); SBAR(); wh[11] = lds_trh(whi1 + 35968); SBAR();
    mfma16_acc(acc[2], cat(wh[2], wh[3]), xf[2]); SBAR(); wh[0] = lds_trh(whi0 + 30912); SBAR();
    mfma16_acc(acc[3], cat(wh[2], wh[3]), xf[3]); SBAR(); wh[1] = lds_trh(whi0 + 36032); SBAR();
    mfma16_acc(acc[4], cat(wh[4], wh[5]), xf[2]); SBAR(); wh[2] = lds_trh(whi1 + 30912); SBAR();
    mfma16_acc(acc[5], cat(wh[4], wh[5]), xf[3]); SBAR(); wh[3] = lds_trh(whi1 + 36032); SBAR();
    mfma16_acc(acc[6], cat(wh[6], wh[7]), xf[2]); SBAR(); wh[4] = lds_trh(whi0 + 30976); SBAR();
    mfma16_acc(acc[7], cat(wh[6], wh[7]), xf[3]); SBAR(); wh[5] = lds_trh(whi0 + 36096); SBAR();
    mfma16_acc(acc[8], cat(wh[8], wh[9]), xf[2]); SBAR(); wh[6] = lds_trh(whi1 + 30976); SBAR();
    mfma16_acc(acc[9], cat(wh[8], wh[9]), xf[3]); SBAR(); wh[7] = lds_trh(whi1 + 36096); SBAR();
    mfma16_acc(acc[10], cat(wh[10], wh[11]), xf[2]); SBAR();
    mfma16_acc(acc[11], cat(wh[10], wh[11]), xf[3]); SBAR();
    mfma16_acc(acc[12], cat(wh[0], wh[1]), xf[2]); SBAR();
    mfma16_acc(acc[13], cat(wh[0], wh[1]), xf[3]); SBAR();
    bar();
    SBAR();
    mfma16_acc(acc[14], cat(wh[2], wh[3]), xf[2]); SBAR(); xf[4] = lds_rd(olo + 32768); wh[8] = lds_trh(wlo0 + 0); SBAR();
    mfma16_acc(acc[15], cat(wh[2], wh[3]), xf[3]); SBAR(); xf[5] = lds_rd(olo + 33792); wh[9] = lds_trh(wlo0 + 5120); SBAR();
    mfma16_acc(acc[16], cat(wh[4], wh[5]), xf[2]); SBAR(); wh[10] = lds_trh(wlo1 + 0); wh[11] = lds_trh(wlo1 + 5120); SBAR();
    mfma16_acc(acc[17], cat(wh[4], wh[5]), xf[3]); SBAR(); wh[0] = lds_trh(wlo0 + 64); wh[1] = lds_trh(wlo0 + 5184); SBAR();
    mfma16_acc(acc[18], cat(wh[6], wh[7]), xf[2]); SBAR(); wh[2] = lds_trh(wlo1 + 64); wh[3] = lds_trh(wlo1 + 5184); SBAR();
    mfma16_acc(acc[19], cat(wh[6], wh[7]), xf[3]); SBAR(); wh[4] = lds_trh(wlo0 + 128); wh[5] = lds_trh(wlo0 + 5248); SBAR();
    mfma16_acc(acc[0], cat(wh[8], wh[9]), xf[4]); SBAR(); wh[6] = lds_trh(wlo1 + 128); SBAR();
    mfma16_acc(acc[1], cat(wh[8], wh[9]), xf[5]); SBAR(); wh[7] = lds_trh(wlo1 + 5248); SBAR();
    mfma16_acc(acc[2], cat(wh[10], wh[11]), xf[4]); SBAR(); wh[8] = lds_trh(wlo0 + 192); SBAR();
    mfma16_acc(acc[3], cat(wh[10], wh[11]), xf[5]); SBAR(); wh[9] = lds_trh(wlo0 + 5312); SBAR();
    mfma16_acc(acc[4], cat(wh[0], wh[1]), xf[4]); SBAR(); wh[10] = lds_trh(wlo1 + 192); SBAR();
    mfma16_acc(acc[5], cat(wh[0], wh[1]), xf[5]); SBAR(); wh[11] = lds_trh(wlo1 + 5312); SBAR();
    mfma16_acc(acc[6], cat(wh[2], wh[3]), xf[4]); SBAR(); xf[6] = lds_rd(olo + 34816); wh[0] = lds_trh(wlo0 + 256); SBAR();
    mfma16_acc(acc[7], cat(wh[2], wh[3]), xf[5]); SBAR(); wh[1] = lds_trh(wlo0 + 5376); SBAR();
    mfma16_acc(acc[8], cat(wh[4], wh[5]), xf[4]); SBAR(); xf[7] = lds_rd(olo + 35840); wh[2] = lds_trh(wlo1 + 256); SBAR();
    mfma16_acc(acc[9], cat(wh[4], wh[5]), xf[5]); SBAR(); wh[3] = lds_trh(wlo1 + 5376); SBAR();
    mfma16_acc(acc[10], cat(wh[6], wh[7]), xf[4]); SBAR(); wh[4] = lds_trh(wlo0 + 10240); SBAR();
    mfma16_acc(acc[11], cat(wh[6], wh[7]), xf[5]); SBAR(); wh[5] = lds_trh(wlo0 + 15360); SBAR();
    mfma16_acc(acc[12], cat(wh[8], wh[9]), xf[4]); SBAR(); wh[6] = lds_trh(wlo1 + 10240); SBAR();
    mfma16_acc(acc[13], cat(wh[8], wh[9]), xf[5]); SBAR(); wh[7] = lds_trh(wlo1 + 15360); SBAR();
    mfma16_acc(acc[14], cat(wh[10], wh[11]), xf[4]); SBAR(); wh[8] = lds_trh(wlo0 + 10304); SBAR();
    mfma16_acc(acc[15], cat(wh[10], wh[11]), xf[5]); SBAR(); wh[9] = lds_trh(wlo0 + 15424); SBAR();
    mfma16_acc(acc[16], cat(wh[0], wh[1]), xf[4]); SBAR(); wh[10] = lds_trh(wlo1 + 10304); SBAR();
    mfma16_acc(acc[17], cat(wh[0], wh[1]), xf[5]); SBAR(); wh[11] = lds_trh(wlo1 + 15424); SBAR();
    mfma16_acc(acc[18], cat(wh[2], wh[3]), xf[4]); SBAR(); wh[0] = lds_trh(wlo0 + 10368); SBAR();
    mfma16_acc(acc[19], cat(wh[2], wh[3]), xf[5]); SBAR(); wh[1] = lds_trh(wlo0 + 15488); SBAR();
    mfma16_acc(acc[0], cat(wh[4], wh[5]), xf[6]); SBAR(); wh[2] = lds_trh(wlo1 + 10368); SBAR();
    mfma16_acc(acc[1], cat(wh[4], wh[5]), xf[7]); SBAR(); wh[3] = lds_trh(wlo1 + 15488); SBAR();
    mfma16_acc(acc[2], cat(wh[6], wh[7]), xf[6]); SBAR(); wh[4] = lds_trh(wlo0 + 10432); SBAR();
    mfma16_acc(acc[3], cat(wh[6], wh[7]), xf[7]); SBAR(); wh[5] = lds_trh(wlo0 + 15552); SBAR();
    mfma16_acc(acc[4], cat(wh[8], wh[9]), xf[6]); SBAR(); wh[6] = lds_trh(wlo1 + 10432); SBAR();
    mfma16_acc(acc[5], cat(wh[8], wh[9]), xf[7]); SBAR(); wh[7] = lds_trh(wlo1 + 15552); SBAR();
    mfma16_acc(acc[6], cat(wh[10], wh[11]), xf[6]); SBAR(); wh[8] = lds_trh(wlo0 + 10496); SBAR();
    mfma16_acc(acc[7], cat(wh[10], wh[11]), xf[7]); SBAR(); wh[9] = lds_trh(wlo0 + 15616); SBAR();
    mfma16_acc(acc[8], cat(wh[0], wh[1]), xf[6]); SBAR(); wh[10] = lds_trh(wlo1 + 10496); SBAR();
    mfma16_acc(acc[9], cat(wh[0], wh[1]), xf[7]); SBAR(); wh[11] = lds_trh(wlo1 + 15616); SBAR();
    mfma16_acc(acc[10], cat(wh[2], wh[3]), xf[6]); SBAR();
    mfma16_acc(acc[11], cat(wh[2], wh[3]), xf[7]); SBAR();
    mfma16_acc(acc[12], cat(wh[4], wh[5]), xf[6]); SBAR();
    mfma16_acc(acc[13], cat(wh[4], wh[5]), xf[7]); SBAR();
    bar();
    SBAR();
    ST(8);
    mfma16_acc(acc[14], cat(wh[6], wh[7]), xf[6]); SBAR(); xf[0] = lds_rd(ohi + 0); wh[0] = lds_trh(wlo0 + 20480); SBAR();
    mfma16_acc(acc[15], cat(wh[6], wh[7]), xf[7]); SBAR(); xf[1] = lds_rd(ohi + 1024); wh[1] = lds_trh(wlo0 + 25600); SBAR();
    mfma16_acc(acc[16], cat(wh[8], wh[9]), xf[6]); SBAR(); wh[2] = lds_trh(wlo1 + 20480); wh[3] = lds_trh(wlo1 + 25600); SBAR();
    mfma16_acc(acc[17], cat(wh[8], wh[9]), xf[7]); SBAR(); wh[4] = lds_trh(wlo0 + 20544); wh[5] = lds_trh(wlo0 + 25664); SBAR();
    mfma16_acc(acc[18], cat(wh[10], wh[11]), xf[6]); SBAR(); wh[6] = lds_trh(wlo1 + 20544); wh[7] = lds_trh(wlo1 + 25664); SBAR();
    mfma16_acc(acc[19], cat(wh[10], wh[11]), xf[7]); SBAR(); wh[8] = lds_trh(wlo0 + 20608); wh[9] = lds_trh(wlo0 + 25728); SBAR();
    mfma16_acc(acc[0], cat(wh[0], wh[1]), xf[0]); SBAR(); wh[10] = lds_trh(wlo1 + 20608); SBAR();
    mfma16_acc(acc[1], cat(wh[0], wh[1]), xf[1]); SBAR(); wh[11] = lds_trh(wlo1 + 25728); SBAR();
    mfma16_acc(acc[2], cat(wh[2], wh[3]), xf[0]); SBAR(); wh[0] = lds_trh(wlo0 + 20672); SBAR();
    mfma16_acc(acc[3], cat(wh[2], wh[3]), xf[1]); SBAR(); wh[1] = lds_trh(wlo0 + 25792); SBAR();
    mfma16_acc(acc[4], cat(wh[4], wh[5]), xf[0]); SBAR(); wh[2] = lds_trh(wlo1 + 20672); SBAR();
    mfma16_acc(acc[5], cat(wh[4], wh[5]), xf[1]); SBAR(); wh[3] = lds_trh(wlo1 + 25792); SBAR();
    mfma16_acc(acc[6], cat(wh[6], wh[7]), xf[0]); SBAR(); xf[2] = lds_rd(ohi + 2048); wh[4] = lds_trh(wlo0 + 20736); SBAR();
    mfma16_acc(acc[7], cat(wh[6], wh[7]), xf[1]); SBAR(); wh[5] = lds_trh(wlo0 + 25856); SBAR();
    mfma16_acc(acc[8], cat(wh[8], wh[9]), xf[0]); SBAR(); xf[3] = lds_rd(ohi + 3072); wh[6] = lds_trh(wlo1 + 20736); SBAR();
    mfma16_acc(acc[9], cat(wh[8], wh[9]), xf[1]); SBAR(); wh[7] = lds_trh(wlo1 + 25856); SBAR();
    mfma16_acc(acc[10], cat(wh[10], wh[11]), xf[0]); SBAR(); wh[8] = lds_trh(wlo0 + 30720); SBAR();
    mfma16_acc(acc[11], cat(wh[10], wh[11]), xf[1]); SBAR(); wh[9] = lds_trh(wlo0 + 35840); SBAR();
    mfma16_acc(acc[12], cat(wh[0], wh[1]), xf[0]); SBAR(); wh[10] = lds_trh(wlo1 + 30720); SBAR();
    mfma16_acc(acc[13], cat(wh[0], wh[1]), xf[1]); SBAR(); wh[11] = lds_trh(wlo1 + 35840); SBAR();
    mfma16_acc(acc[14], cat(wh[2], wh[3]), xf[0]); SBAR(); wh[0] = lds_trh(wlo0 + 30784); SBAR();
    mfma16_acc(acc[15], cat(wh[2], wh[3]), xf[1]); SBAR(); wh[1] = lds_trh(wlo0 + 35904); SBAR();
    mfma16_acc(acc[16], cat(wh[4], wh[5]), xf[0]); SBAR(); wh[2] = lds_trh(wlo1 + 30784); SBAR();
    mfma16_acc(acc[17], cat(wh[4], wh[5]), xf[1]); SBAR(); wh[3] = lds_trh(wlo1 + 35904); SBAR();
    mfma16_acc(acc[18], cat(wh[6], wh[7]), xf[0]); SBAR(); wh[4] = lds_trh(wlo0 + 30848); SBAR();
    mfma16_acc(acc[19], cat(wh[6], wh[7]), xf[1]); SBAR(); wh[5] = lds_trh(wlo0 + 35968); SBAR();
    mfma16_acc(acc[0], cat(wh[8], wh[9]), xf[2]); SBAR(); wh[6] = lds_trh(wlo1 + 30848); SBAR();
    mfma16_acc(acc[1], cat(wh[8], wh[9]), xf[3]); SBAR(); wh[7] = lds_trh(wlo1 + 35968); SBAR();
    mfma16_acc(acc[2], cat(wh[10], wh[11]), xf[2]); SBAR(); wh[8] = lds_trh(wlo0 + 30912); SBAR();
    mfma16_acc(acc[3], cat(wh[10], wh[11]), xf[3]); SBAR(); wh[9] = lds_trh(wlo0 + 36032); SBAR();
    mfma16_acc(acc[4], cat(wh[0], wh[1]), xf[2]); SBAR(); wh[10] = lds_trh(wlo1 + 30912); SBAR();
    mfma16_acc(acc[5], cat(wh[0], wh[1]), xf[3]); SBAR(); wh[11] = lds_trh(wlo1 + 36032); SBAR();
    mfma16_acc(acc[6], cat(wh[2], wh[3]), xf[2]); SBAR(); wh[0] = lds_trh(wlo0 + 30976); SBAR();
    mfma16_acc(acc[7], cat(wh[2], wh[3]), xf[3]); SBAR(); wh[1] = lds_trh(wlo0 + 36096); SBAR();
    mfma16_acc(acc[8], cat(wh[4], wh[5]), xf[2]); SBAR(); wh[2] = lds_trh(wlo1 + 30976); SBAR();
    mfma16_acc(acc[9], cat(wh[4], wh[5]), xf[3]); SBAR(); wh[3] = lds_trh(wlo1 + 36096); SBAR();
    mfma16_acc(acc[10], cat(wh[6], wh[7]), xf[2]); SBAR();
    mfma16_acc(acc[11], cat(wh[6], wh[7]), xf[3]); SBAR();
    mfma16_acc(acc[12], cat(wh[8], wh[9]), xf[2]); SBAR();
    mfma16_acc(acc[13], cat(wh[8], wh[9]), xf[3]); SBAR();
    bar();
    SBAR();
    mfma16_acc(acc[14], cat(wh[10], wh[11]), xf[2]); SBAR(); xf[4] = lds_rd(ohi + 16384); wh[4] = lds_trh(whi0 + 0); SBAR();
    mfma16_acc(acc[15], cat(wh[10], wh[11]), xf[3]); SBAR(); xf[5] = lds_rd(ohi + 17408); wh[5] = lds_trh(whi0 + 5120); SBAR();
    mfma16_acc(acc[16], cat(wh[0], wh[1]), xf[2]); SBAR(); wh[6] = lds_trh(whi1 + 0); wh[7] = lds_trh(whi1 + 5120); SBAR();
    mfma16_acc(acc[17], cat(wh[0], wh[1]), xf[3]); SBAR(); wh[8] = lds_trh(whi0 + 64); wh[9] = lds_trh(whi0 + 5184); SBAR();
    mfma16_acc(acc[18], cat(wh[2], wh[3]), xf[2]); SBAR(); wh[10] = lds_trh(whi1 + 64); wh[11] = lds_trh(whi1 + 5184); SBAR();
    mfma16_acc(acc[19], cat(wh[2], wh[3]), xf[3]); SBAR(); wh[0] = lds_trh(whi0 + 128); wh[1] = lds_trh(whi0 + 5248); SBAR();
    mfma16_acc(acc[0], cat(wh[4], wh[5]), xf[4]); SBAR(); wh[2] = lds_trh(whi1 + 128); SBAR();
    mfma16_acc(acc[1], cat(wh[4], wh[5]), xf[5]); SBAR(); wh[3] = lds_trh(whi1 + 5248); SBAR();
    mfma16_acc(acc[2], cat(wh[6], wh[7]), xf[4]); SBAR(); wh[4] = lds_trh(whi0 + 192); SBAR();
    mfma16_acc(acc[3], cat(wh[6], wh[7]), xf[5]); SBAR(); wh[5] = lds_trh(whi0 + 5312); SBAR();
    mfma16_acc(acc[4], cat(wh[8], wh[9]), xf[4]); SBAR(); wh[6] = lds_trh(whi1 + 192); SBAR();
    mfma16_acc(acc[5], cat(wh[8], wh[9]), xf[5]); SBAR(); wh[7] = lds_trh(whi1 + 5312); SBAR();
    mfma16_acc(acc[6], cat(wh[10], wh[11]), xf[4]); SBAR(); xf[6] = lds_rd(ohi + 18432); wh[8] = lds_trh(whi0 + 256); SBAR();
    mfma16_acc(acc[7], cat(wh[10], wh[11]), xf[5]); SBAR(); wh[9] = lds_trh(whi0 + 5376); SBAR();
    mfma16_acc(acc[8], cat(wh[0], wh[1]), xf[4]); SBAR(); xf[7] = lds_rd(ohi + 19456); wh[10] = lds_trh(whi1 + 256); SBAR();
    mfma16_acc(acc[9], cat(wh[0], wh[1]), xf[5]); SBAR(); wh[11] = lds_trh(whi1 + 5376); SBAR();
    mfma16_acc(acc[10], cat(wh[2], wh[3]), xf[4]); SBAR(); wh[0] = lds_trh(whi0 + 10240); SBAR();
    mfma16_acc(acc[11], cat(wh[2], wh[3]), xf[5]); SBAR(); wh[1] = lds_trh(whi0 + 15360); SBAR();
    mfma16_acc(acc[12], cat(wh[4], wh[5]), xf[4]); SBAR(); wh[2] = lds_trh(whi1 + 10240); SBAR();
    mfma16_acc(acc[13], cat(wh[4], wh[5]), xf[5]); SBAR(); wh[3] = lds_trh(whi1 + 15360); SBAR();
    mfma16_acc(acc[14], cat(wh[6], wh[7]), xf[4]); SBAR(); wh[4] = lds_trh(whi0 + 10304); SBAR();
    mfma16_acc(acc[15], cat(wh[6], wh[7]), xf[5]); SBAR(); wh[5] = lds_trh(whi0 + 15424); SBAR();
    mfma16_acc(acc[16], cat(wh[8], wh[9]), xf[4]); SBAR(); wh[6] = lds_trh(whi1 + 10304); SBAR();
    mfma16_acc(acc[17], cat(wh[8], wh[9]), xf[5]); SBAR(); wh[7] = lds_trh(whi1 + 15424); SBAR();
    mfma16_acc(acc[18], cat(wh[10], wh[11]), xf[4]); SBAR(); wh[8] = lds_trh(whi0 + 10368); SBAR();
    mfma16_acc(acc[19], cat(wh[10], wh[11]), xf[5]); SBAR(); wh[9] = lds_trh(whi0 + 15488); SBAR();
    mfma16_acc(acc[0], cat(wh[0], wh[1]), xf[6]); SBAR(); wh[10] = lds_trh(whi1 + 10368); SBAR();
    mfma16_acc(acc[1], cat(wh[0], wh[1]), xf[7]); SBAR(); wh[11] = lds_trh(whi1 + 15488); SBAR();
    mfma16_acc(acc[2], cat(wh[2], wh[3]), xf[6]); SBAR(); wh[0] = lds_trh(whi0 + 10432); SBAR();
    mfma16_acc(acc[3], cat(wh[2], wh[3]), xf[7]); SBAR(); wh[1] = lds_trh(whi0 + 15552); SBAR();
    mfma16_acc(acc[4], cat(wh[4], wh[5]), xf[6]); SBAR(); wh[2] = lds_trh(whi1 + 10432); SBAR();
    mfma16_acc(acc[5], cat(wh[4], wh[5]), xf[7]); SBAR(); wh[3] = lds_trh(whi1 + 15552); SBAR();
    mfma16_acc(acc[6], cat(wh[6], wh[7]), xf[6]); SBAR(); wh[4] = lds_trh(whi0 + 10496); SBAR();
    mfma16_acc(acc[7], cat(wh[6], wh[7]), xf[7]); SBAR(); wh[5] = lds_trh(whi0 + 15616); SBAR();
    mfma16_acc(acc[8], cat(wh[8], wh[9]), xf[6]); SBAR(); wh[6] = lds_trh(whi1 + 10496); SBAR();
    mfma16_acc(acc[9], cat(wh[8], wh[9]), xf[7]); SBAR(); wh[7] = lds_trh(whi1 + 15616); SBAR();
    mfma16_acc(acc[10], cat(wh[10], wh[11]), xf[6]); SBAR();
    mfma16_acc(acc[11], cat(wh[10], wh[11]), xf[7]); SBAR();
    mfma16_acc(acc[12], cat(wh[0], wh[1]), xf[6]); SBAR();
    mfma16_acc(acc[13], cat(wh[0], wh[1]), xf[7]); SBAR();
    bar();
    SBAR();
    mfma16_acc(acc[14], cat(wh[2], wh[3]), xf[6]); SBAR(); xf[0] = lds_rd(olo + 0); wh[8] = lds_trh(whi0 + 20480); SBAR();
    mfma16_acc(acc[15], cat(wh[2], wh[3]), xf[7]); SBAR(); xf[1] = lds_rd(olo + 1024); wh[9] = lds_trh(whi0 + 25600); SBAR();
    mfma16_acc(acc[16], cat(wh[4], wh[5]), xf[6]); SBAR(); wh[10] = lds_trh(whi1 + 20480); wh[11] = lds_trh(whi1 + 25600); SBAR();
    mfma16_acc(acc[17], cat(wh[4], wh[5]), xf[7]); SBAR(); wh[0] = lds_trh(whi0 + 20544); wh[1] = lds_trh(whi0 + 25664); SBAR();
    mfma16_acc(acc[18], cat(wh[6], wh[7]), xf[6]); SBAR(); wh[2] = lds_trh(whi1 + 20544); wh[3] = lds_trh(whi1 + 25664); SBAR();
    mfma16_acc(acc[19], cat(wh[6], wh[7]), xf[7]); SBAR(); wh[4] = lds_trh(whi0 + 20608); wh[5] = lds_trh(whi0 + 25728); SBAR();
    mfma16_acc(acc[0], cat(wh[8], wh[9]), xf[0]); SBAR(); wh[6] = lds_trh(whi1 + 20608); SBAR();
    mfma16_acc(acc[1], cat(wh[8], wh[9]), xf[1]); SBAR(); wh[7] = lds_trh(whi1 + 25728); SBAR();
    mfma16_acc(acc[2], cat(wh[10], wh[11]), xf[0]); SBAR(); wh[8] = lds_trh(whi0 + 20672); SBAR();
    mfma16_acc(acc[3], cat(wh[10], wh[11]), xf[1]); SBAR(); wh[9] = lds_trh(whi0 + 25792); SBAR();
    mfma16_acc(acc[4], cat(wh[0], wh[1]), xf[0]); SBAR(); wh[10] = lds_trh(whi1 + 20672); SBAR();
    mfma16_acc(acc[5], cat(wh[0], wh[1]), xf[1]); SBAR(); wh[11] = lds_trh(whi1 + 25792); SBAR();
    mfma16_acc(acc[6], cat(wh[2], wh[3]), xf[0]); SBAR(); xf[2] = lds_rd(olo + 2048); wh[0] = lds_trh(whi0 + 20736); SBAR();
    mfma16_acc(acc[7], cat(wh[2], wh[3]), xf[1]); SBAR(); wh[1] = lds_trh(whi0 + 25856); SBAR();
    mfma16_acc(acc[8], cat(wh[4], wh[5]), xf[0]); SBAR(); xf[3] = lds_rd(olo + 3072); wh[2] = lds_trh(whi1 + 20736); SBAR();
    mfma16_acc(acc[9], cat(wh[4], wh[5]), xf[1]); SBAR(); wh[3] = lds_trh(whi1 + 25856); SBAR();
    mfma16_acc(acc[10], cat(wh[6], wh[7]), xf[0]); SBAR(); wh[4] = lds_trh(whi0 + 30720); SBAR();
    mfma16_acc(acc[11], cat(wh[6], wh[7]), xf[1]); SBAR(); wh[5] = lds_trh(whi0 + 35840); SBAR();
    mfma16_acc(acc[12], cat(wh[8], wh[9]), xf[0]); SBAR(); wh[6] = lds_trh(whi1 + 30720); SBAR();
    mfma16_acc(acc[13], cat(wh[8], wh[9]), xf[1]); SBAR(); wh[7] = lds_trh(whi1 + 35840); SBAR();
    mfma16_acc(acc[14], cat(wh[10], wh[11]), xf[0]); SBAR(); wh[8] = lds_trh(whi0 + 30784); SBAR();
    mfma16_acc(acc[15], cat(wh[10], wh[11]), xf[1]); SBAR(); wh[9] = lds_trh(whi0 + 35904); SBAR();
    mfma16_acc(acc[16], cat(wh[0], wh[1]), xf[0]); SBAR(); wh[10] = lds_trh(whi1 + 30784); SBAR();
    mfma16_acc(acc[17], cat(wh[0], wh[1]), xf[1]); SBAR(); wh[11] = lds_trh(whi1 + 35904); SBAR();
    mfma16_acc(acc[18], cat(wh[2], wh[3]), xf[0]); SBAR(); wh[0] = lds_trh(whi0 + 30848); SBAR();
    mfma16_acc(acc[19], cat(wh[2], wh[3]), xf[1]); SBAR(); wh[1] = lds_trh(whi0 + 35968); SBAR();
    mfma16_acc(acc[0], cat(wh[4], wh[5]), xf[2]); SBAR(); wh[2] = lds_trh(whi1 + 30848); SBAR();
    mfma16_acc(acc[1], cat(wh[4], wh[5]), xf[3]); SBAR(); wh[3] = lds_trh(whi1 + 35968); SBAR();
    mfma16_acc(acc[2], cat(wh[6], wh[7]), xf[2]); SBAR(); wh[4] = lds_trh(whi0 + 30912); SBAR();
    mfma16_acc(acc[3], cat(wh[6], wh[7]), xf[3]); SBAR(); wh[5] = lds_trh(whi0 + 36032); SBAR();
    mfma16_acc(acc[4], cat(wh[8], wh[9]), xf[2]); SBAR(); wh[6] = lds_trh(whi1 + 30912); SBAR();
    mfma16_acc(acc[5], cat(wh[8], wh[9]), xf[3]); SBAR(); wh[7] = lds_trh(whi1 + 36032); SBAR();
    mfma16_acc(acc[6], cat(wh[10], wh[11]), xf[2]); SBAR(); wh[8] = lds_trh(whi0 + 30976); SBAR();
    mfma16_acc(acc[7], cat(wh[10], wh[11]), xf[3]); SBAR(); wh[9] = lds_trh(whi0 + 36096); SBAR();
    mfma16_acc(acc[8], cat(wh[0], wh[1]), xf[2]); SBAR(); wh[10] = lds_trh(whi1 + 30976); SBAR();
    mfma16_acc(acc[9], cat(wh[0], wh[1]), xf[3]); SBAR(); wh[11] = lds_trh(whi1 + 36096); SBAR();
    mfma16_acc(acc[10], cat(wh[2], wh[3]), xf[2]); SBAR();
    mfma16_acc(acc[11], cat(wh[2], wh[3]), xf[3]); SBAR();
    mfma16_acc(acc[12], cat(wh[4], wh[5]), xf[2]); SBAR();
    mfma16_acc(acc[13], cat(wh[4], wh[5]), xf[3]); SBAR();
    bar();
    SBAR();
    ST(9);
    mfma16_acc(acc[14], cat(wh[6], wh[7]), xf[2]); SBAR(); xf[4] = lds_rd(olo + 16384); wh[0] = lds_trh(wlo0 + 0); SBAR();
    mfma16_acc(acc[15], cat(wh[6], wh[7]), xf[3]); SBAR(); xf[5] = lds_rd(olo + 17408); wh[1] = lds_trh(wlo0 + 5120); SBAR();
    mfma16_acc(acc[16], cat(wh[8], wh[9]), xf[2]); SBAR(); wh[2] = lds_trh(wlo1 + 0); wh[3] = lds_trh(wlo1 + 5120); SBAR();
    mfma16_acc(acc[17], cat(wh[8], wh[9]), xf[3]); SBAR(); wh[4] = lds_trh(wlo0 + 64); wh[5] = lds_trh(wlo0 + 5184); SBAR();
    mfma16_acc(acc[18], cat(wh[10], wh[11]), xf[2]); SBAR(); wh[6] = lds_trh(wlo1 + 64); wh[7] = lds_trh(wlo1 + 5184); SBAR();
    mfma16_acc(acc[19], cat(wh[10], wh[11]), xf[3]); SBAR(); wh[8] = lds_trh(wlo0 + 128); wh[9] = lds_trh(wlo0 + 5248); SBAR();
    mfma16_acc(acc[0], cat(wh[0], wh[1]), xf[4]); SBAR(); wh[10] = lds_trh(wlo1 + 128); SBAR();
    mfma16_acc(acc[1], cat(wh[0], wh[1]), xf[5]); SBAR(); wh[11] = lds_trh(wlo1 + 5248); SBAR();
    mfma16_acc(acc[2], cat(wh[2], wh[3]), xf[4]); SBAR(); wh[0] = lds_trh(wlo0 + 192); SBAR();
    mfma16_acc(acc[3], cat(wh[2], wh[3]), xf[5]); SBAR(); wh[1] = lds_trh(wlo0 + 5312); SBAR();
    mfma16_acc(acc[4], cat(wh[4], wh[5]), xf[4]); SBAR(); wh[2] = lds_trh(wlo1 + 192); SBAR();
    mfma16_acc(acc[5], cat(wh[4], wh[5]), xf[5]); SBAR(); wh[3] = lds_trh(wlo1 + 5312); SBAR();
    mfma16_acc(acc[6], cat(wh[6], wh[7]), xf[4]); SBAR(); xf[6] = lds_rd(olo + 18432); wh[4] = lds_trh(wlo0 + 256); SBAR();
    mfma16_acc(acc[7], cat(wh[6], wh[7]), xf[5]); SBAR(); wh[5] = lds_trh(wlo0 + 5376); SBAR();
    mfma16_acc(acc[8], cat(wh[8], wh[9]), xf[4]); SBAR(); xf[7] = lds_rd(olo + 19456); wh[6] = lds_trh(wlo1 + 256); SBAR();
    mfma16_acc(acc[9], cat(wh[8], wh[9]), xf[5]); SBAR(); wh[7] = lds_trh(wlo1 + 5376); SBAR();
    mfma16_acc(acc[10], cat(wh[10], wh[11]), xf[4]); SBAR(); wh[8] = lds_trh(wlo0 + 10240); SBAR();
    mfma16_acc(acc[11], cat(wh[10], wh[11]), xf[5]); SBAR(); wh[9] = lds_trh(wlo0 + 15360); SBAR();
    mfma16_acc(acc[12], cat(wh[0], wh[1]), xf[4]); SBAR(); wh[10] = lds_trh(wlo1 + 10240); SBAR();
    mfma16_acc(acc[13], cat(wh[0], wh[1]), xf[5]); SBAR(); wh[11] = lds_trh(wlo1 + 15360); SBAR();
    mfma16_acc(acc[14], cat(wh[2], wh[3]), xf[4]); SBAR(); wh[0] = lds_trh(wlo0 + 10304); SBAR();
    mfma16_acc(acc[15], cat(wh[2], wh[3]), xf[5]); SBAR(); wh[1] = lds_trh(wlo0 + 15424); SBAR();
    mfma16_acc(acc[16], cat(wh[4], wh[5]), xf[4]); SBAR(); wh[2] = lds_trh(wlo1 + 10304); SBAR();
    mfma16_acc(acc[17], cat(wh[4], wh[5]), xf[5]); SBAR(); wh[3] = lds_trh(wlo1 + 15424); SBAR();
    mfma16_acc(acc[18], cat(wh[6], wh[7]), xf[4]); SBAR(); wh[4] = lds_trh(wlo0 + 10368); SBAR();
    mfma16_acc(acc[19], cat(wh[6], wh[7]), xf[5]); SBAR(); wh[5] = lds_trh(wlo0 + 15488); SBAR();
    mfma16_acc(acc[0], cat(wh[8], wh[9]), xf[6]); SBAR(); wh[6] = lds_trh(wlo1 + 10368); SBAR();
    mfma16_acc(acc[1], cat(wh[8], wh[9]), xf[7]); SBAR(); wh[7] = lds_trh(wlo1 + 15488); SBAR();
    mfma16_acc(acc[2], cat(wh[10], wh[11]), xf[6]); SBAR(); wh[8] = lds_trh(wlo0 + 10432); SBAR();
    mfma16_acc(acc[3], cat(wh[10], wh[11]), xf[7]); SBAR(); wh[9] = lds_trh(wlo0 + 15552); SBAR();
    mfma16_acc(acc[4], cat(wh[0], wh[1]), xf[6]); SBAR(); wh[10] = lds_trh(wlo1 + 10432); SBAR();
    mfma16_acc(acc[5], cat(wh[0], wh[1]), xf[7]); SBAR(); wh[11] = lds_trh(wlo1 + 15552); SBAR();
    mfma16_acc(acc[6], cat(wh[2], wh[3]), xf[6]); SBAR(); wh[0] = lds_trh(wlo0 + 10496); SBAR();
    mfma16_acc(acc[7], cat(wh[2], wh[3]), xf[7]); SBAR(); wh[1] = lds_trh(wlo0 + 15616); SBAR();
    mfma16_acc(acc[8], cat(wh[4], wh[5]), xf[6]); SBAR(); wh[2] = lds_trh(wlo1 + 10496); SBAR();
    mfma16_acc(acc[9], cat(wh[4], wh[5]), xf[7]); SBAR(); wh[3] = lds_trh(wlo1 + 15616); SBAR();
    mfma16_acc(acc[10], cat(wh[6], wh[7]), xf[6]); SBAR();
    mfma16_acc(acc[11], cat(wh[6], wh[7]), xf[7]); SBAR();
    mfma16_acc(acc[12], cat(wh[8], wh[9]), xf[6]); SBAR();
    mfma16_acc(acc[13], cat(wh[8], wh[9]), xf[7]); SBAR();
    bar();
    SBAR();
    mfma16_acc(acc[14], cat(wh[10], wh[11]), xf[6]); SBAR(); xf[0] = lds_rd(olo + 32768); wh[4] = lds_trh(wlo0 + 20480); SBAR();
    mfma16_acc(acc[15], cat(wh[10], wh[11]), xf[7]); SBAR(); xf[1] = lds_rd(olo + 33792); wh[5] = lds_trh(wlo0 + 25600); SBAR();
    mfma16_acc(acc[16], cat(wh[0], wh[1]), xf[6]); SBAR(); wh[6] = lds_trh(wlo1 + 20480); wh[7] = lds_trh(wlo1 + 25600); SBAR();
    mfma16_acc(acc[17], cat(wh[0], wh[1]), xf[7]); SBAR(); wh[8] = lds_trh(wlo0 + 20544); wh[9] = lds_trh(wlo0 + 25664); SBAR();
    mfma16_acc(acc[18], cat(wh[2], wh[3]), xf[6]); SBAR(); wh[10] = lds_trh(wlo1 + 20544); wh[11] = lds_trh(wlo1 + 25664); SBAR();
    mfma16_acc(acc[19], cat(wh[2], wh[3]), xf[7]); SBAR(); wh[0] = lds_trh(wlo0 + 20608); wh[1] = lds_trh(wlo0 + 25728); SBAR();
    mfma16_acc(acc[0], cat(wh[4], wh[5]), xf[0]); SBAR(); wh[2] = lds_trh(wlo1 + 20608); SBAR();
    mfma16_acc(acc[1], cat(wh[4], wh[5]), xf[1]); SBAR(); wh[3] = lds_trh(wlo1 + 25728); SBAR();
    mfma16_acc(acc[2], cat(wh[6], wh[7]), xf[0]); SBAR(); wh[4] = lds_trh(wlo0 + 20672); SBAR();
    mfma16_acc(acc[3], cat(wh[6], wh[7]), xf[1]); SBAR(); wh[5] = lds_trh(wlo0 + 25792); SBAR();
    mfma16_acc(acc[4], cat(wh[8], wh[9]), xf[0]); SBAR(); wh[6] = lds_trh(wlo1 + 20672); SBAR();
    mfma16_acc(acc[5], cat(wh[8], wh[9]), xf[1]); SBAR(); wh[7] = lds_trh(wlo1 + 25792); SBAR();
    mfma16_acc(acc[6], cat(wh[10], wh[11]), xf[0]); SBAR(); xf[2] = lds_rd(olo + 34816); wh[8] = lds_trh(wlo0 + 20736); SBAR();
    mfma16_acc(acc[7], cat(wh[10], wh[11]), xf[1]); SBAR(); wh[9] = lds_trh(wlo0 + 25856); SBAR();
    mfma16_acc(acc[8], cat(wh[0], wh[1]), xf[0]); SBAR(); xf[3] = lds_rd(olo + 35840); wh[10] = lds_trh(wlo1 + 20736); SBAR();
    mfma16_acc(acc[9], cat(wh[0], wh[1]), xf[1]); SBAR(); wh[11] = lds_trh(wlo1 + 25856); SBAR();
    mfma16_acc(acc[10], cat(wh[2], wh[3]), xf[0]); SBAR(); wh[0] = lds_trh(wlo0 + 30720); SBAR();
    mfma16_acc(acc[11], cat(wh[2], wh[3]), xf[1]); SBAR(); wh[1] = lds_trh(wlo0 + 35840); SBAR();
    mfma16_acc(acc[12], cat(wh[4], wh[5]), xf[0]); SBAR(); wh[2] = lds_trh(wlo1 + 30720); SBAR();
    mfma16_acc(acc[13], cat(wh[4], wh[5]), xf[1]); SBAR(); wh[3] = lds_trh(wlo1 + 35840); SBAR();
    mfma16_acc(acc[14], cat(wh[6], wh[7]), xf[0]); SBAR(); wh[4] = lds_trh(wlo0 + 30784); SBAR();
    mfma16_acc(acc[15], cat(wh[6], wh[7]), xf[1]); SBAR(); wh[5] = lds_trh(wlo0 + 35904); SBAR();
    mfma16_acc(acc[16], cat(wh[8], wh[9]), xf[0]); SBAR(); wh[6] = lds_trh(wlo1 + 30784); SBAR();
    mfma16_acc(acc[17], cat(wh[8], wh[9]), xf[1]); SBAR(); wh[7] = lds_trh(wlo1 + 35904); SBAR();
    mfma16_acc(acc[18], cat(wh[10], wh[11]), xf[0]); SBAR(); wh[8] = lds_trh(wlo0 + 30848); SBAR();
    mfma16_acc(acc[19], cat(wh[10], wh[11]), xf[1]); SBAR(); wh[9] = lds_trh(wlo0 + 35968); SBAR();
    mfma16_acc(acc[0], cat(wh[0], wh[1]), xf[2]); SBAR(); wh[10] = lds_trh(wlo1 + 30848); SBAR();
    mfma16_acc(acc[1], cat(wh[0], wh[1]), xf[3]); SBAR(); wh[11] = lds_trh(wlo1 + 35968); SBAR();
    mfma16_acc(acc[2], cat(wh[2], wh[3]), xf[2]); SBAR(); wh[0] = lds_trh(wlo0 + 30912); SBAR();
    mfma16_acc(acc[3], cat(wh[2], wh[3]), xf[3]); SBAR(); wh[1] = lds_trh(wlo0 + 36032); SBAR();
    mfma16_acc(acc[4], cat(wh[4], wh[5]), xf[2]); SBAR(); wh[2] = lds_trh(wlo1 + 30912); SBAR();
    mfma16_acc(acc[5], cat(wh[4], wh[5]), xf[3]); SBAR(); wh[3] = lds_trh(wlo1 + 36032); SBAR();
    mfma16_acc(acc[6], cat(wh[6], wh[7]), xf[2]); SBAR(); wh[4] = lds_trh(wlo0 + 30976); SBAR();
    mfma16_acc(acc[7], cat(wh[6], wh[7]), xf[3]); SBAR(); wh[5] = lds_trh(wlo0 + 36096); SBAR();
    mfma16_acc(acc[8], cat(wh[8], wh[9]), xf[2]); SBAR(); wh[6] = lds_trh(wlo1 + 30976); SBAR();
    mfma16_acc(acc[9], cat(wh[8], wh[9]), xf[3]); SBAR(); wh[7] = lds_trh(wlo1 + 36096); SBAR();
    mfma16_acc(acc[10], cat(wh[10], wh[11]), xf[2]); SBAR();
    mfma16_acc(acc[11], cat(wh[10], wh[11]), xf[3]); SBAR();
    mfma16_acc(acc[12], cat(wh[0], wh[1]), xf[2]); SBAR();
    mfma16_acc(acc[13], cat(wh[0], wh[1]), xf[3]); SBAR();
    bar();
    SBAR();
    mfma16_acc(acc[14], cat(wh[2], wh[3]), xf[2]); SBAR(); xf[4] = lds_rd(ohi + 0); wh[8] = lds_trh(whi0 + 0); SBAR();
    mfma16_acc(acc[15], cat(wh[2], wh[3]), xf[3]); SBAR(); xf[5] = lds_rd(ohi + 1024); wh[9] = lds_trh(whi0 + 5120); SBAR();
    mfma16_acc(acc[16], cat(wh[4], wh[5]), xf[2]); SBAR(); wh[10] = lds_trh(whi1 + 0); wh[11] = lds_trh(whi1 + 5120); SBAR();
    mfma16_acc(acc[17], cat(wh[4], wh[5]), xf[3]); SBAR(); wh[0] = lds_trh(whi0 + 64); wh[1] = lds_trh(whi0 + 5184); SBAR();
    mfma16_acc(acc[18], cat(wh[6], wh[7]), xf[2]); SBAR(); wh[2] = lds_trh(whi1 + 64); wh[3] = lds_trh(whi1 + 5184); SBAR();
    mfma16_acc(acc[19], cat(wh[6], wh[7]), xf[3]); SBAR(); wh[4] = lds_trh(whi0 + 128); wh[5] = lds_trh(whi0 + 5248); SBAR();
    mfma16_acc(acc[0], cat(wh[8], wh[9]), xf[4]); SBAR(); wh[6] = lds_trh(whi1 + 128); SBAR();
    mfma16_acc(acc[1], cat(wh[8], wh[9]), xf[5]); SBAR(); wh[7] = lds_trh(whi1 + 5248); SBAR();
    mfma16_acc(acc[2], cat(wh[10], wh[11]), xf[4]); SBAR(); wh[8] = lds_trh(whi0 + 192); SBAR();
    mfma16_acc(acc[3], cat(wh[10], wh[11]), xf[5]); SBAR(); wh[9] = lds_trh(whi0 + 5312); SBAR();
    mfma16_acc(acc[4], cat(wh[0], wh[1]), xf[4]); SBAR(); wh[10] = lds_trh(whi1 + 192); SBAR();
    mfma16_acc(acc[5], cat(wh[0], wh[1]), xf[5]); SBAR(); wh[11] = lds_trh(whi1 + 5312); SBAR();
    mfma16_acc(acc[6], cat(wh[2], wh[3]), xf[4]); SBAR(); xf[6] = lds_rd(ohi + 2048); wh[0] = lds_trh(whi0 + 256); SBAR();
    mfma16_acc(acc[7], cat(wh[2], wh[3]), xf[5]); SBAR(); wh[1] = lds_trh(whi0 + 5376); SBAR();
    mfma16_acc(acc[8], cat(wh[4], wh[5]), xf[4]); SBAR(); xf[7] = lds_rd(ohi + 3072); wh[2] = lds_trh(whi1 + 256); SBAR();
    mfma16_acc(acc[9], cat(wh[4], wh[5]), xf[5]); SBAR(); wh[3] = lds_trh(whi1 + 5376); SBAR();
    mfma16_acc(acc[10], cat(wh[6], wh[7]), xf[4]); SBAR(); wh[4] = lds_trh(whi0 + 10240); SBAR();
    mfma16_acc(acc[11], cat(wh[6], wh[7]), xf[5]); SBAR(); wh[5] = lds_trh(whi0 + 15360); SBAR();
    mfma16_acc(acc[12], cat(wh[8], wh[9]), xf[4]); SBAR(); wh[6] = lds_trh(whi1 + 10240); SBAR();
    mfma16_acc(acc[13], cat(wh[8], wh[9]), xf[5]); SBAR(); wh[7] = lds_trh(whi1 + 15360); SBAR();
    mfma16_acc(acc[14], cat(wh[10], wh[11]), xf[4]); SBAR(); wh[8] = lds_trh(whi0 + 10304); SBAR();
    mfma16_acc(acc[15], cat(wh[10], wh[11]), xf[5]); SBAR(); wh[9] = lds_trh(whi0 + 15424); SBAR();
    mfma16_acc(acc[16], cat(wh[0], wh[1]), xf[4]); SBAR(); wh[10] = lds_trh(whi1 + 10304); SBAR();
    mfma16_acc(acc[17], cat(wh[0], wh[1]), xf[5]); SBAR(); wh[11] = lds_trh(whi1 + 15424); SBAR();
    mfma16_acc(acc[18], cat(wh[2], wh[3]), xf[4]); SBAR(); wh[0] = lds_trh(whi0 + 10368); SBAR();
    mfma16_acc(acc[19], cat(wh[2], wh[3]), xf[5]); SBAR(); wh[1] = lds_trh(whi0 + 15488); SBAR();
    mfma16_acc(acc[0], cat(wh[4], wh[5]), xf[6]); SBAR(); wh[2] = lds_trh(whi1 + 10368); SBAR();
    mfma16_acc(acc[1], cat(wh[4], wh[5]), xf[7]); SBAR(); wh[3] = lds_trh(whi1 + 15488); SBAR();
    mfma16_acc(acc[2], cat(wh[6], wh[7]), xf[6]); SBAR(); wh[4] = lds_trh(whi0 + 10432); SBAR();
    mfma16_acc(acc[3], cat(wh[6], wh[7]), xf[7]); SBAR(); wh[5] = lds_trh(whi0 + 15552); SBAR();
    mfma16_acc(acc[4], cat(wh[8], wh[9]), xf[6]); SBAR(); wh[6] = lds_trh(whi1 + 10432); SBAR();
    mfma16_acc(acc[5], cat(wh[8], wh[9]), xf[7]); SBAR(); wh[7] = lds_trh(whi1 + 15552); SBAR();
    mfma16_acc(acc[6], cat(wh[10], wh[11]), xf[6]); SBAR(); wh[8] = lds_trh(whi0 + 10496); SBAR();
    mfma16_acc(acc[7], cat(wh[10], wh[11]), xf[7]); SBAR(); wh[9] = lds_trh(whi0 + 15616); SBAR();
    mfma16_acc(acc[8], cat(wh[0], wh[1]), xf[6]); SBAR(); wh[10] = lds_trh(whi1 + 10496); SBAR();
    mfma16_acc(acc[9], cat(wh[0], wh[1]), xf[7]); SBAR(); wh[11] = lds_trh(whi1 + 15616); SBAR();
    mfma16_acc(acc[10], cat(wh[2], wh[3]), xf[6]); SBAR();
    mfma16_acc(acc[11], cat(wh[2], wh[3]), xf[7]); SBAR();
    mfma16_acc(acc[12], cat(wh[4], wh[5]), xf[6]); SBAR();
    mfma16_acc(acc[13], cat(wh[4], wh[5]), xf[7]); SBAR();
    bar();
    SBAR();
    ST(10);
    mfma16_acc(acc[14], cat(wh[6], wh[7]), xf[6]); SBAR(); xf[0] = lds_rd(ohi + 16384); wh[0] = lds_trh(whi0 + 20480); SBAR();
    mfma16_acc(acc[15], cat(wh[6], wh[7]), xf[7]); SBAR(); xf[1] = lds_rd(ohi + 17408); wh[1] = lds_trh(whi0 + 25600); SBAR();
    mfma16_acc(acc[16], cat(wh[8], wh[9]), xf[6]); SBAR(); wh[2] = lds_trh(whi1 + 20480); wh[3] = lds_trh(whi1 + 25600); SBAR();
    mfma16_acc(acc[17], cat(wh[8], wh[9]), xf[7]); SBAR(); wh[4] = lds_trh(whi0 + 20544); wh[5] = lds_trh(whi0 + 25664); SBAR();
    mfma16_acc(acc[18], cat(wh[10], wh[11]), xf[6]); SBAR(); wh[6] = lds_trh(whi1 + 20544); wh[7] = lds_trh(whi1 + 25664); SBAR();
    mfma16_acc(acc[19], cat(wh[10], wh[11]), xf[7]); SBAR(); wh[8] = lds_trh(whi0 + 20608); wh[9] = lds_trh(whi0 + 25728); SBAR();
    mfma16_acc(acc[0], cat(wh[0], wh[1]), xf[0]); SBAR(); wh[10] = lds_trh(whi1 + 20608); SBAR();
    mfma16_acc(acc[1], cat(wh[0], wh[1]), xf[1]); SBAR(); wh[11] = lds_trh(whi1 + 25728); SBAR();
    mfma16_acc(acc[2], cat(wh[2], wh[3]), xf[0]); SBAR(); wh[0] = lds_trh(whi0 + 20672); SBAR();
    mfma16_acc(acc[3], cat(wh[2], wh[3]), xf[1]); SBAR(); wh[1] = lds_trh(whi0 + 25792); SBAR();
    mfma16_acc(acc[4], cat(wh[4], wh[5]), xf[0]); SBAR(); wh[2] = lds_trh(whi1 + 20672); SBAR();
    mfma16_acc(acc[5], cat(wh[4], wh[5]), xf[1]); SBAR(); wh[3] = lds_trh(whi1 + 25792); SBAR();
    mfma16_acc(acc[6], cat(wh[6], wh[7]), xf[0]); SBAR(); xf[2] = lds_rd(ohi + 18432); wh[4] = lds_trh(whi0 + 20736); SBAR();
    mfma16_acc(acc[7], cat(wh[6], wh[7]), xf[1]); SBAR(); wh[5] = lds_trh(whi0 + 25856); SBAR();
    mfma16_acc(acc[8], cat(wh[8], wh[9]), xf[0]); SBAR(); xf[3] = lds_rd(ohi + 19456); wh[6] = lds_trh(whi1 + 20736); SBAR();
    mfma16_acc(acc[9], cat(wh[8], wh[9]), xf[1]); SBAR(); wh[7] = lds_trh(whi1 + 25856); SBAR();
    mfma16_acc(acc[10], cat(wh[10], wh[11]), xf[0]); SBAR(); wh[8] = lds_trh(whi0 + 30720); SBAR();
    mfma16_acc(acc[11], cat(wh[10], wh[11]), xf[1]); SBAR(); wh[9] = lds_trh(whi0 + 35840); SBAR();
    mfma16_acc(acc[12], cat(wh[0], wh[1]), xf[0]); SBAR(); wh[10] = lds_trh(whi1 + 30720); SBAR();
    mfma16_acc(acc[13], cat(wh[0], wh[1]), xf[1]); SBAR(); wh[11] = lds_trh(whi1 + 35840); SBAR();
    mfma16_acc(acc[14], cat(wh[2], wh[3]), xf[0]); SBAR(); wh[0] = lds_trh(whi0 + 30784); SBAR();
    mfma16_acc(acc[15], cat(wh[2], wh[3]), xf[1]); SBAR(); wh[1] = lds_trh(whi0 + 35904); SBAR();
    mfma16_acc(acc[16], cat(wh[4], wh[5]), xf[0]); SBAR(); wh[2] = lds_trh(whi1 + 30784); SBAR();
    mfma16_acc(acc[17], cat(wh[4], wh[5]), xf[1]); SBAR(); wh[3] = lds_trh(whi1 + 35904); SBAR();
    mfma16_acc(acc[18], cat(wh[6], wh[7]), xf[0]); SBAR(); wh[4] = lds_trh(whi0 + 30848); SBAR();
    mfma16_acc(acc[19], cat(wh[6], wh[7]), xf[1]); SBAR(); wh[5] = lds_trh(whi0 + 35968); SBAR();
    mfma16_acc(acc[0], cat(wh[8], wh[9]), xf[2]); SBAR(); wh[6] = lds_trh(whi1 + 30848); SBAR();
    mfma16_acc(acc[1], cat(wh[8], wh[9]), xf[3]); SBAR(); wh[7] = lds_trh(whi1 + 35968); SBAR();
    mfma16_acc(acc[2], cat(wh[10], wh[11]), xf[2]); SBAR(); wh[8] = lds_trh(whi0 + 30912); SBAR();
    mfma16_acc(acc[3], cat(wh[10], wh[11]), xf[3]); SBAR(); wh[9] = lds_trh(whi0 + 36032); SBAR();
    mfma16_acc(acc[4], cat(wh[0], wh[1]), xf[2]); SBAR(); wh[10] = lds_trh(whi1 + 30912); SBAR();
    mfma16_acc(acc[5], cat(wh[0], wh[1]), xf[3]); SBAR(); wh[11] = lds_trh(whi1 + 36032); SBAR();
    mfma16_acc(acc[6], cat(wh[2], wh[3]), xf[2]); SBAR(); wh[0] = lds_trh(whi0 + 30976); SBAR();
    mfma16_acc(acc[7], cat(wh[2], wh[3]), xf[3]); SBAR(); wh[1] = lds_trh(whi0 + 36096); SBAR();
    mfma16_acc(acc[8], cat(wh[4], wh[5]), xf[2]); SBAR(); wh[2] = lds_trh(whi1 + 30976); SBAR();
    mfma16_acc(acc[9], cat(wh[4], wh[5]), xf[3]); SBAR(); wh[3] = lds_trh(whi1 + 36096); SBAR();
    mfma16_acc(acc[10], cat(wh[6], wh[7]), xf[2]); SBAR();
    mfma16_acc(acc[11], cat(wh[6], wh[7]), xf[3]); SBAR();
    mfma16_acc(acc[12], cat(wh[8], wh[9]), xf[2]); SBAR();
    mfma16_acc(acc[13], cat(wh[8], wh[9]), xf[3]); SBAR();
    bar();
    SBAR();
    mfma16_acc(acc[14], cat(wh[10], wh[11]), xf[2]); SBAR(); xf[4] = lds_rd(olo + 0); wh[4] = lds_trh(wlo0 + 0); SBAR();
    mfma16_acc(acc[15], cat(wh[10], wh[11]), xf[3]); SBAR(); xf[5] = lds_rd(olo + 1024); wh[5] = lds_trh(wlo0 + 5120); SBAR();
    mfma16_acc(acc[16], cat(wh[0], wh[1]), xf[2]); SBAR(); wh[6] = lds_trh(wlo1 + 0); wh[7] = lds_trh(wlo1 + 5120); SBAR();
    mfma16_acc(acc[17], cat(wh[0], wh[1]), xf[3]); SBAR(); wh[8] = lds_trh(wlo0 + 64); wh[9] = lds_trh(wlo0 + 5184); SBAR();
    mfma16_acc(acc[18], cat(wh[2], wh[3]), xf[2]); SBAR(); wh[10] = lds_trh(wlo1 + 64); wh[11] = lds_trh(wlo1 + 5184); SBAR();
    mfma16_acc(acc[19], cat(wh[2], wh[3]), xf[3]); SBAR(); wh[0] = lds_trh(wlo0 + 128); wh[1] = lds_trh(wlo0 + 5248); SBAR();
    mfma16_acc(acc[0], cat(wh[4], wh[5]), xf[4]); SBAR(); wh[2] = lds_trh(wlo1 + 128); SBAR();
    mfma16_acc(acc[1], cat(wh[4], wh[5]), xf[5]); SBAR(); wh[3] = lds_trh(wlo1 + 5248); SBAR();
    mfma16_acc(acc[2], cat(wh[6], wh[7]), xf[4]); SBAR(); wh[4] = lds_trh(wlo0 + 192); SBAR();
    mfma16_acc(acc[3], cat(wh[6], wh[7]), xf[5]); SBAR(); wh[5] = lds_trh(wlo0 + 5312); SBAR();
    mfma16_acc(acc[4], cat(wh[8], wh[9]), xf[4]); SBAR(); wh[6] = lds_trh(wlo1 + 192); SBAR();
    mfma16_acc(acc[5], cat(wh[8], wh[9]), xf[5]); SBAR(); wh[7] = lds_trh(wlo1 + 5312); SBAR();
    mfma16_acc(acc[6], cat(wh[10], wh[11]), xf[4]); SBAR(); xf[6] = lds_rd(olo + 2048); wh[8] = lds_trh(wlo0 + 256); SBAR();
    mfma16_acc(acc[7], cat(wh[10], wh[11]), xf[5]); SBAR(); wh[9] = lds_trh(wlo0 + 5376); SBAR();
    mfma16_acc(acc[8], cat(wh[0], wh[1]), xf[4]); SBAR(); xf[7] = lds_rd(olo + 3072); wh[10] = lds_trh(wlo1 + 256); SBAR();
    mfma16_acc(acc[9], cat(wh[0], wh[1]), xf[5]); SBAR(); wh[11] = lds_trh(wlo1 + 5376); SBAR();
    mfma16_acc(acc[10], cat(wh[2], wh[3]), xf[4]); SBAR(); wh[0] = lds_trh(wlo0 + 10240); SBAR();
    mfma16_acc(acc[11], cat(wh[2], wh[3]), xf[5]); SBAR(); wh[1] = lds_trh(wlo0 + 15360); SBAR();
    mfma16_acc(acc[12], cat(wh[4], wh[5]), xf[4]); SBAR(); wh[2] = lds_trh(wlo1 + 10240); SBAR();
    mfma16_acc(acc[13], cat(wh[4], wh[5]), xf[5]); SBAR(); wh[3] = lds_trh(wlo1 + 15360); SBAR();
    mfma16_acc(acc[14], cat(wh[6], wh[7]), xf[4]); SBAR(); wh[4] = lds_trh(wlo0 + 10304); SBAR();
    mfma16_acc(acc[15], cat(wh[6], wh[7]), xf[5]); SBAR(); wh[5] = lds_trh(wlo0 + 15424); SBAR();
    mfma16_acc(acc[16], cat(wh[8], wh[9]), xf[4]); SBAR(); wh[6] = lds_trh(wlo1 + 10304); SBAR();
    mfma16_acc(acc[17], cat(wh[8], wh[9]), xf[5]); SBAR(); wh[7] = lds_trh(wlo1 + 15424); SBAR();
    mfma16_acc(acc[18], cat(wh[10], wh[11]), xf[4]); SBAR(); wh[8] = lds_trh(wlo0 + 10368); SBAR();
    mfma16_acc(acc[19], cat(wh[10], wh[11]), xf[5]); SBAR(); wh[9] = lds_trh(wlo0 + 15488); SBAR();
    mfma16_acc(acc[0], cat(wh[0], wh[1]), xf[6]); SBAR(); wh[10] = lds_trh(wlo1 + 10368); SBAR();
    mfma16_acc(acc[1], cat(wh[0], wh[1]), xf[7]); SBAR(); wh[11] = lds_trh(wlo1 + 15488); SBAR();
    mfma16_acc(acc[2], cat(wh[2], wh[3]), xf[6]); SBAR(); wh[0] = lds_trh(wlo0 + 10432); SBAR();
    mfma16_acc(acc[3], cat(wh[2], wh[3]), xf[7]); SBAR(); wh[1] = lds_trh(wlo0 + 15552); SBAR();
    mfma16_acc(acc[4], cat(wh[4], wh[5]), xf[6]); SBAR(); wh[2] = lds_trh(wlo1 + 10432); SBAR();
    mfma16_acc(acc[5], cat(wh[4], wh[5]), xf[7]); SBAR(); wh[3] = lds_trh(wlo1 + 15552); SBAR();
    mfma16_acc(acc[6], cat(wh[6], wh[7]), xf[6]); SBAR(); wh[4] = lds_trh(wlo0 + 10496); SBAR();
    mfma16_acc(acc[7], cat(wh[6], wh[7]), xf[7]); SBAR(); wh[5] = lds_trh(wlo0 + 15616); SBAR();
    mfma16_acc(acc[8], cat(wh[8], wh[9]), xf[6]); SBAR(); wh[6] = lds_trh(wlo1 + 10496); SBAR();
    mfma16_acc(acc[9], cat(wh[8], wh[9]), xf[7]); SBAR(); wh[7] = lds_trh(wlo1 + 15616); SBAR();
    mfma16_acc(acc[10], cat(wh[10], wh[11]), xf[6]); SBAR();
    mfma16_acc(acc[11], cat(wh[10], wh[11]), xf[7]); SBAR();
    mfma16_acc(acc[12], cat(wh[0], wh[1]), xf[6]); SBAR();
    mfma16_acc(acc[13], cat(wh[0], wh[1]), xf[7]); SBAR();
    bar();
    SBAR();
    mfma16_acc(acc[14], cat(wh[2], wh[3]), xf[6]); SBAR(); xf[0] = lds_rd(olo + 16384); wh[8] = lds_trh(wlo0 + 20480); SBAR();
    mfma16_acc(acc[15], cat(wh[2], wh[3]), xf[7]); SBAR(); xf[1] = lds_rd(olo + 17408); wh[9] = lds_trh(wlo0 + 25600); SBAR();
    mfma16_acc(acc[16], cat(wh[4], wh[5]), xf[6]); SBAR(); wh[10] = lds_trh(wlo1 + 20480); wh[11] = lds_trh(wlo1 + 25600); SBAR();
    mfma16_acc(acc[17], cat(wh[4], wh[5]), xf[7]); SBAR(); wh[0] = lds_trh(wlo0 + 20544); wh[1] = lds_trh(wlo0 + 25664); SBAR();
    mfma16_acc(acc[18], cat(wh[6], wh[7]), xf[6]); SBAR(); wh[2] = lds_trh(wlo1 + 20544); wh[3] = lds_trh(wlo1 + 25664); SBAR();
    mfma16_acc(acc[19], cat(wh[6], wh[7]), xf[7]); SBAR(); wh[4] = lds_trh(wlo0 + 20608); wh[5] = lds_trh(wlo0 + 25728); SBAR();
    mfma16_acc(acc[0], cat(wh[8], wh[9]), xf[0]); SBAR(); wh[6] = lds_trh(wlo1 + 20608); SBAR();
    mfma16_acc(acc[1], cat(wh[8], wh[9]), xf[1]); SBAR(); wh[7] = lds_trh(wlo1 + 25728); SBAR();
    mfma16_acc(acc[2], cat(wh[10], wh[11]), xf[0]); SBAR(); wh[8] = lds_trh(wlo0 + 20672); SBAR();
    mfma16_acc(acc[3], cat(wh[10], wh[11]), xf[1]); SBAR(); wh[9] = lds_trh(wlo0 + 25792); SBAR();
    mfma16_acc(acc[4], cat(wh[0], wh[1]), xf[0]); SBAR(); wh[10] = lds_trh(wlo1 + 20672); SBAR();
    mfma16_acc(acc[5], cat(wh[0], wh[1]), xf[1]); SBAR(); wh[11] = lds_trh(wlo1 + 25792); SBAR();
    mfma16_acc(acc[6], cat(wh[2], wh[3]), xf[0]); SBAR(); xf[2] = lds_rd(olo + 18432); wh[0] = lds_trh(wlo0 + 20736); SBAR();
    mfma16_acc(acc[7], cat(wh[2], wh[3]), xf[1]); SBAR(); wh[1] = lds_trh(wlo0 + 25856); SBAR();
    mfma16_acc(acc[8], cat(wh[4], wh[5]), xf[0]); SBAR(); xf[3] = lds_rd(olo + 19456); wh[2] = lds_trh(wlo1 + 20736); SBAR();
    mfma16_acc(acc[9], cat(wh[4], wh[5]), xf[1]); SBAR(); wh[3] = lds_trh(wlo1 + 25856); SBAR();
    mfma16_acc(acc[10], cat(wh[6], wh[7]), xf[0]); SBAR(); wh[4] = lds_trh(wlo0 + 30720); SBAR();
    mfma16_acc(acc[11], cat(wh[6], wh[7]), xf[1]); SBAR(); wh[5] = lds_trh(wlo0 + 35840); SBAR();
    mfma16_acc(acc[12], cat(wh[8], wh[9]), xf[0]); SBAR(); wh[6] = lds_trh(wlo1 + 30720); SBAR();
    mfma16_acc(acc[13], cat(wh[8], wh[9]), xf[1]); SBAR(); wh[7] = lds_trh(wlo1 + 35840); SBAR();
    mfma16_acc(acc[14], cat(wh[10], wh[11]), xf[0]); SBAR(); wh[8] = lds_trh(wlo0 + 30784); SBAR();
    mfma16_acc(acc[15], cat(wh[10], wh[11]), xf[1]); SBAR(); wh[9] = lds_trh(wlo0 + 35904); SBAR();
    mfma16_acc(acc[16], cat(wh[0], wh[1]), xf[0]); SBAR(); wh[10] = lds_trh(wlo1 + 30784); SBAR();
    mfma16_acc(acc[17], cat(wh[0], wh[1]), xf[1]); SBAR(); wh[11] = lds_trh(wlo1 + 35904); SBAR();
    mfma16_acc(acc[18], cat(wh[2], wh[3]), xf[0]); SBAR(); wh[0] = lds_trh(wlo0 + 30848); SBAR();
    mfma16_acc(acc[19], cat(wh[2], wh[3]), xf[1]); SBAR(); wh[1] = lds_trh(wlo0 + 35968); SBAR();
    mfma16_acc(acc[0], cat(wh[4], wh[5]), xf[2]); SBAR(); wh[2] = lds_trh(wlo1 + 30848); SBAR();
    mfma16_acc(acc[1], cat(wh[4], wh[5]), xf[3]); SBAR(); wh[3] = lds_trh(wlo1 + 35968); SBAR();
    mfma16_acc(acc[2], cat(wh[6], wh[7]), xf[2]); SBAR(); wh[4] = lds_trh(wlo0 + 30912); SBAR();
    mfma16_acc(acc[3], cat(wh[6], wh[7]), xf[3]); SBAR(); wh[5] = lds_trh(wlo0 + 36032); SBAR();
    mfma16_acc(acc[4], cat(wh[8], wh[9]), xf[2]); SBAR(); wh[6] = lds_trh(wlo1 + 30912); SBAR();
    mfma16_acc(acc[5], cat(wh[8], wh[9]), xf[3]); SBAR(); wh[7] = lds_trh(wlo1 + 36032); SBAR();
    mfma16_acc(acc[6], cat(wh[10], wh[11]), xf[2]); SBAR(); wh[8] = lds_trh(wlo0 + 30976); SBAR();
    mfma16_acc(acc[7], cat(wh[10], wh[11]), xf[3]); SBAR(); wh[9] = lds_trh(wlo0 + 36096); SBAR();
    mfma16_acc(acc[8], cat(wh[0], wh[1]), xf[2]); SBAR(); wh[10] = lds_trh(wlo1 + 30976); SBAR();
    mfma16_acc(acc[9], cat(wh[0], wh[1]), xf[3]); SBAR(); wh[11] = lds_trh(wlo1 + 36096); SBAR();
    mfma16_acc(acc[10], cat(wh[2], wh[3]), xf[2]); SBAR();
    mfma16_acc(acc[11], cat(wh[2], wh[3]), xf[3]); SBAR();
    mfma16_acc(acc[12], cat(wh[4], wh[5]), xf[2]); SBAR();
    mfma16_acc(acc[13], cat(wh[4], wh[5]), xf[3]); SBAR();
    bar();
    SBAR();
    ST(11);
    mfma16_acc(acc[14], cat(wh[6], wh[7]), xf[2]); SBAR(); xf[4] = lds_rd(olo + 32768); wh[0] = lds_trh(whi0 + 0); SBAR();
    mfma16_acc(acc[15], cat(wh[6], wh[7]), xf[3]); SBAR(); xf[5] = lds_rd(olo + 33792); wh[1] = lds_trh(whi0 + 5120); SBAR();
    mfma16_acc(acc[16], cat(wh[8], wh[9]), xf[2]); SBAR(); wh[2] = lds_trh(whi1 + 0); wh[3] = lds_trh(whi1 + 5120); SBAR();
    mfma16_acc(acc[17], cat(wh[8], wh[9]), xf[3]); SBAR(); wh[4] = lds_trh(whi0 + 64); wh[5] = lds_trh(whi0 + 5184); SBAR();
    mfma16_acc(acc[18], cat(wh[10], wh[11]), xf[2]); SBAR(); wh[6] = lds_trh(whi1 + 64); wh[7] = lds_trh(whi1 + 5184); SBAR();
    mfma16_acc(acc[19], cat(wh[10], wh[11]), xf[3]); SBAR(); wh[8] = lds_trh(whi0 + 128); wh[9] = lds_trh(whi0 + 5248); SBAR();
    mfma16_acc(acc[0], cat(wh[0], wh[1]), xf[4]); SBAR(); wh[10] = lds_trh(whi1 + 128); SBAR();
    mfma16_acc(acc[1], cat(wh[0], wh[1]), xf[5]); SBAR(); wh[11] = lds_trh(whi1 + 5248); SBAR();
    mfma16_acc(acc[2], cat(wh[2], wh[3]), xf[4]); SBAR(); wh[0] = lds_trh(whi0 + 192); SBAR();
    mfma16_acc(acc[3], cat(wh[2], wh[3]), xf[5]); SBAR(); wh[1] = lds_trh(whi0 + 5312); SBAR();
    mfma16_acc(acc[4], cat(wh[4], wh[5]), xf[4]); SBAR(); wh[2] = lds_trh(whi1 + 192); SBAR();
    mfma16_acc(acc[5], cat(wh[4], wh[5]), xf[5]); SBAR(); wh[3] = lds_trh(whi1 + 5312); SBAR();
    mfma16_acc(acc[6], cat(wh[6], wh[7]), xf[4]); SBAR(); xf[6] = lds_rd(olo + 34816); wh[4] = lds_trh(whi0 + 256); SBAR();
    mfma16_acc(acc[7], cat(wh[6], wh[7]), xf[5]); SBAR(); wh[5] = lds_trh(whi0 + 5376); SBAR();
    mfma16_acc(acc[8], cat(wh[8], wh[9]), xf[4]); SBAR(); xf[7] = lds_rd(olo + 35840); wh[6] = lds_trh(whi1 + 256); SBAR();
    mfma16_acc(acc[9], cat(wh[8], wh[9]), xf[5]); SBAR(); wh[7] = lds_trh(whi1 + 5376); SBAR();
    mfma16_acc(acc[10], cat(wh[10], wh[11]), xf[4]); SBAR(); wh[8] = lds_trh(whi0 + 10240); SBAR();
    mfma16_acc(acc[11], cat(wh[10], wh[11]), xf[5]); SBAR(); wh[9] = lds_trh(whi0 + 15360); SBAR();
    mfma16_acc(acc[12], cat(wh[0], wh[1]), xf[4]); SBAR(); wh[10] = lds_trh(whi1 + 10240); SBAR();
    mfma16_acc(acc[13], cat(wh[0], wh[1]), xf[5]); SBAR(); wh[11] = lds_trh(whi1 + 15360); SBAR();
    mfma16_acc(acc[14], cat(wh[2], wh[3]), xf[4]); SBAR(); wh[0] = lds_trh(whi0 + 10304); SBAR();
    mfma16_acc(acc[15], cat(wh[2], wh[3]), xf[5]); SBAR(); wh[1] = lds_trh(whi0 + 15424); SBAR();
    mfma16_acc(acc[16], cat(wh[4], wh[5]), xf[4]); SBAR(); wh[2] = lds_trh(whi1 + 10304); SBAR();
    mfma16_acc(acc[17], cat(wh[4], wh[5]), xf[5]); SBAR(); wh[3] = lds_trh(whi1 + 15424); SBAR();
    mfma16_acc(acc[18], cat(wh[6], wh[7]), xf[4]); SBAR(); wh[4] = lds_trh(whi0 + 10368); SBAR();
    mfma16_acc(acc[19], cat(wh[6], wh[7]), xf[5]); SBAR(); wh[5] = lds_trh(whi0 + 15488); SBAR();
    mfma16_acc(acc[0], cat(wh[8], wh[9]), xf[6]); SBAR(); wh[6] = lds_trh(whi1 + 10368); SBAR();
    mfma16_acc(acc[1], cat(wh[8], wh[9]), xf[7]); SBAR(); wh[7] = lds_trh(whi1 + 15488); SBAR();
    mfma16_acc(acc[2], cat(wh[10], wh[11]), xf[6]); SBAR(); wh[8] = lds_trh(whi0 + 10432); SBAR();
    mfma16_acc(acc[3], cat(wh[10], wh[11]), xf[7]); SBAR(); wh[9] = lds_trh(whi0 + 15552); SBAR();
    mfma16_acc(acc[4], cat(wh[0], wh[1]), xf[6]); SBAR(); wh[10] = lds_trh(whi1 + 10432); SBAR();
    mfma16_acc(acc[5], cat(wh[0], wh[1]), xf[7]); SBAR(); wh[11] = lds_trh(whi1 + 15552); SBAR();
    mfma16_acc(acc[6], cat(wh[2], wh[3]), xf[6]); SBAR(); wh[0] = lds_trh(whi0 + 10496); SBAR();
    mfma16_acc(acc[7], cat(wh[2], wh[3]), xf[7]); SBAR(); wh[1] = lds_trh(whi0 + 15616); SBAR();
    mfma16_acc(acc[8], cat(wh[4], wh[5]), xf[6]); SBAR(); wh[2] = lds_trh(whi1 + 10496); SBAR();
    mfma16_acc(acc[9], cat(wh[4], wh[5]), xf[7]); SBAR(); wh[3] = lds_trh(whi1 + 15616); SBAR();
    mfma16_acc(acc[10], cat(wh[6], wh[7]), xf[6]); SBAR();
    mfma16_acc(acc[11], cat(wh[6], wh[7]), xf[7]); SBAR();
    mfma16_acc(acc[12], cat(wh[8], wh[9]), xf[6]); SBAR();
    mfma16_acc(acc[13], cat(wh[8], wh[9]), xf[7]); SBAR();
    bar();
    SBAR();
    mfma16_acc(acc[14], cat(wh[10], wh[11]), xf[6]); SBAR(); xf[0] = lds_rd(ohi + 0); wh[4] = lds_trh(whi0 + 20480); SBAR();
    mfma16_acc(acc[15], cat(wh[10], wh[11]), xf[7]); SBAR(); xf[1] = lds_rd(ohi + 1024); wh[5] = lds_trh(whi0 + 25600); SBAR();
    mfma16_acc(acc[16], cat(wh[0], wh[1]), xf[6]); SBAR(); wh[6] = lds_trh(whi1 + 20480); wh[7] = lds_trh(whi1 + 25600); SBAR();
    mfma16_acc(acc[17], cat(wh[0], wh[1]), xf[7]); SBAR(); wh[8] = lds_trh(whi0 + 20544); wh[9] = lds_trh(whi0 + 25664); SBAR();
    mfma16_acc(acc[18], cat(wh[2], wh[3]), xf[6]); SBAR(); wh[10] = lds_trh(whi1 + 20544); wh[11] = lds_trh(whi1 + 25664); SBAR();
    mfma16_acc(acc[19], cat(wh[2], wh[3]), xf[7]); SBAR(); wh[0] = lds_trh(whi0 + 20608); wh[1] = lds_trh(whi0 + 25728); SBAR();
    mfma16_acc(acc[0], cat(wh[4], wh[5]), xf[0]); SBAR(); wh[2] = lds_trh(whi1 + 20608); SBAR();
    mfma16_acc(acc[1], cat(wh[4], wh[5]), xf[1]); SBAR(); wh[3] = lds_trh(whi1 + 25728); SBAR();
    mfma16_acc(acc[2], cat(wh[6], wh[7]), xf[0]); SBAR(); wh[4] = lds_trh(whi0 + 20672); SBAR();
    mfma16_acc(acc[3], cat(wh[6], wh[7]), xf[1]); SBAR(); wh[5] = lds_trh(whi0 + 25792); SBAR();
    mfma16_acc(acc[4], cat(wh[8], wh[9]), xf[0]); SBAR(); wh[6] = lds_trh(whi1 + 20672); SBAR();
    mfma16_acc(acc[5], cat(wh[8], wh[9]), xf[1]); SBAR(); wh[7] = lds_trh(whi1 + 25792); SBAR();
    mfma16_acc(acc[6], cat(wh[10], wh[11]), xf[0]); SBAR(); xf[2] = lds_rd(ohi + 2048); wh[8] = lds_trh(whi0 + 20736); SBAR();
    mfma16_acc(acc[7], cat(wh[10], wh[11]), xf[1]); SBAR(); wh[9] = lds_trh(whi0 + 25856); SBAR();
    mfma16_acc(acc[8], cat(wh[0], wh[1]), xf[0]); SBAR(); xf[3] = lds_rd(ohi + 3072); wh[10] = lds_trh(whi1 + 20736); SBAR();
    mfma16_acc(acc[9], cat(wh[0], wh[1]), xf[1]); SBAR(); wh[11] = lds_trh(whi1 + 25856); SBAR();
    mfma16_acc(acc[10], cat(wh[2], wh[3]), xf[0]); SBAR(); wh[0] = lds_trh(whi0 + 30720); SBAR();
    mfma16_acc(acc[11], cat(wh[2], wh[3]), xf[1]); SBAR(); wh[1] = lds_trh(whi0 + 35840); SBAR();
    mfma16_acc(acc[12], cat(wh[4], wh[5]), xf[0]); SBAR(); wh[2] = lds_trh(whi1 + 30720); SBAR();
    mfma16_acc(acc[13], cat(wh[4], wh[5]), xf[1]); SBAR(); wh[3] = lds_trh(whi1 + 35840); SBAR();
    mfma16_acc(acc[14], cat(wh[6], wh[7]), xf[0]); SBAR(); wh[4] = lds_trh(whi0 + 30784); SBAR();
    mfma16_acc(acc[15], cat(wh[6], wh[7]), xf[1]); SBAR(); wh[5] = lds_trh(whi0 + 35904); SBAR();
    mfma16_acc(acc[16], cat(wh[8], wh[9]), xf[0]); SBAR(); wh[6] = lds_trh(whi1 + 30784); SBAR();
    mfma16_acc(acc[17], cat(wh[8], wh[9]), xf[1]); SBAR(); wh[7] = lds_trh(whi1 + 35904); SBAR();
    mfma16_acc(acc[18], cat(wh[10], wh[11]), xf[0]); SBAR(); wh[8] = lds_trh(whi0 + 30848); SBAR();
    mfma16_acc(acc[19], cat(wh[10], wh[11]), xf[1]); SBAR(); wh[9] = lds_trh(whi0 + 35968); SBAR();
    mfma16_acc(acc[0], cat(wh[0], wh[1]), xf[2]); SBAR(); wh[10] = lds_trh(whi1 + 30848); SBAR();
    mfma16_acc(acc[1], cat(wh[0], wh[1]), xf[3]); SBAR(); wh[11] = lds_trh(whi1 + 35968); SBAR();
    mfma16_acc(acc[2], cat(wh[2], wh[3]), xf[2]); SBAR(); wh[0] = lds_trh(whi0 + 30912); SBAR();
    mfma16_acc(acc[3], cat(wh[2], wh[3]), xf[3]); SBAR(); wh[1] = lds_trh(whi0 + 36032); SBAR();
    mfma16_acc(acc[4], cat(wh[4], wh[5]), xf[2]); SBAR(); wh[2] = lds_trh(whi1 + 30912); SBAR();
    mfma16_acc(acc[5], cat(wh[4], wh[5]), xf[3]); SBAR(); wh[3] = lds_trh(whi1 + 36032); SBAR();
    mfma16_acc(acc[6], cat(wh[6], wh[7]), xf[2]); SBAR(); wh[4] = lds_trh(whi0 + 30976); SBAR();
    mfma16_acc(acc[7], cat(wh[6], wh[7]), xf[3]); SBAR(); wh[5] = lds_trh(whi0 + 36096); SBAR();
    mfma16_acc(acc[8], cat(wh[8], wh[9]), xf[2]); SBAR(); wh[6] = lds_trh(whi1 + 30976); SBAR();
    mfma16_acc(acc[9], cat(wh[8], wh[9]), xf[3]); SBAR(); wh[7] = lds_trh(whi1 + 36096); SBAR();
    mfma16_acc(acc[10], cat(wh[10], wh[11]), xf[2]); SBAR();
    mfma16_acc(acc[11], cat(wh[10], wh[11]), xf[3]); SBAR();
    mfma16_acc(acc[12], cat(wh[0], wh[1]), xf[2]); SBAR();
    mfma16_acc(acc[13], cat(wh[0], wh[1]), xf[3]); SBAR();
    bar();
    SBAR();
    mfma16_acc(acc[14], cat(wh[2], wh[3]), xf[2]); SBAR(); xf[4] = lds_rd(ohi + 16384); wh[8] = lds_trh(wlo0 + 0); SBAR();
    mfma16_acc(acc[15], cat(wh[2], wh[3]), xf[3]); SBAR(); xf[5] = lds_rd(ohi + 17408); wh[9] = lds_trh(wlo0 + 5120); SBAR();
    mfma16_acc(acc[16], cat(wh[4], wh[5]), xf[2]); SBAR(); xf[6] = lds_rd(ohi + 18432); wh[10] = lds_trh(wlo1 + 0); SBAR();
    mfma16_acc(acc[17], cat(wh[4], wh[5]), xf[3]); SBAR(); xf[7] = lds_rd(ohi + 19456); wh[11] = lds_trh(wlo1 + 5120); SBAR();
    mfma16_acc(acc[18], cat(wh[6], wh[7]), xf[2]); SBAR(); wh[0] = lds_trh(wlo0 + 10240); wh[1] = lds_trh(wlo0 + 15360); SBAR();
    mfma16_acc(acc[19], cat(wh[6], wh[7]), xf[3]); SBAR(); wh[2] = lds_trh(wlo1 + 10240); wh[3] = lds_trh(wlo1 + 15360); SBAR();
    mfma16_acc(acc[0], cat(wh[8], wh[9]), xf[4]); SBAR(); wh[4] = lds_trh(wlo0 + 64); wh[5] = lds_trh(wlo0 + 5184); SBAR();
    mfma16_acc(acc[1], cat(wh[8], wh[9]), xf[5]); SBAR(); wh[6] = lds_trh(wlo1 + 64); SBAR();
    mfma16_acc(acc[2], cat(wh[10], wh[11]), xf[4]); SBAR(); wh[7] = lds_trh(wlo1 + 5184); wh[8] = lds_trh(wlo0 + 10304); SBAR();
    mfma16_acc(acc[3], cat(wh[10], wh[11]), xf[5]); SBAR(); wh[9] = lds_trh(wlo0 + 15424); SBAR();
    mfma16_acc(acc[0], cat(wh[0], wh[1]), xf[6]); SBAR(); wh[10] = lds_trh(wlo1 + 10304); SBAR();
    mfma16_acc(acc[1], cat(wh[0], wh[1]), xf[7]); SBAR(); wh[11] = lds_trh(wlo1 + 15424); SBAR();
    mfma16_acc(acc[2], cat(wh[2], wh[3]), xf[6]); SBAR(); wh[0] = lds_trh(wlo0 + 128); SBAR();
    mfma16_acc(acc[3], cat(wh[2], wh[3]), xf[7]); SBAR(); wh[1] = lds_trh(wlo0 + 5248); SBAR();
    mfma16_acc(acc[4], cat(wh[4], wh[5]), xf[4]); SBAR(); wh[2] = lds_trh(wlo1 + 128); SBAR();
    mfma16_acc(acc[5], cat(wh[4], wh[5]), xf[5]); SBAR(); wh[3] = lds_trh(wlo1 + 5248); SBAR();
    mfma16_acc(acc[6], cat(wh[6], wh[7]), xf[4]); SBAR(); wh[4] = lds_trh(wlo0 + 10368); SBAR();
    mfma16_acc(acc[7], cat(wh[6], wh[7]), xf[5]); SBAR(); wh[5] = lds_trh(wlo0 + 15488); SBAR();
    mfma16_acc(acc[4], cat(wh[8], wh[9]), xf[6]); SBAR(); wh[6] = lds_trh(wlo1 + 10368); SBAR();
    mfma16_acc(acc[5], cat(wh[8], wh[9]), xf[7]); SBAR(); wh[7] = lds_trh(wlo1 + 15488); SBAR();
    mfma16_acc(acc[6], cat(wh[10], wh[11]), xf[6]); SBAR(); wh[8] = lds_trh(wlo0 + 192); SBAR();
    mfma16_acc(acc[7], cat(wh[10], wh[11]), xf[7]); SBAR(); wh[9] = lds_trh(wlo0 + 5312); SBAR(); PIN(acc[0]); PIN(acc[1]); PIN(acc[2]); PIN(acc[3]); FIN(0);
    mfma16_acc(acc[8], cat(wh[0], wh[1]), xf[4]); SBAR(); wh[10] = lds_trh(wlo1 + 192); SBAR();
    mfma16_acc(acc[9], cat(wh[0], wh[1]), xf[5]); SBAR(); wh[11] = lds_trh(wlo1 + 5312); SBAR();
    mfma16_acc(acc[10], cat(wh[2], wh[3]), xf[4]); SBAR(); wh[0] = lds_trh(wlo0 + 10432); SBAR();
    mfma16_acc(acc[11], cat(wh[2], wh[3]), xf[5]); SBAR(); wh[1] = lds_trh(wlo0 + 15552); SBAR();
    mfma16_acc(acc[8], cat(wh[4], wh[5]), xf[6]); SBAR(); wh[2] = lds_trh(wlo1 + 10432); SBAR();
    mfma16_acc(acc[9], cat(wh[4], wh[5]), xf[7]); SBAR(); wh[3] = lds_trh(wlo1 + 15552); SBAR();
    mfma16_acc(acc[10], cat(wh[6], wh[7]), xf[6]); SBAR(); wh[4] = lds_trh(wlo0 + 256); SBAR();
    mfma16_acc(acc[11], cat(wh[6], wh[7]), xf[7]); SBAR(); wh[5] = lds_trh(wlo0 + 5376); SBAR(); PIN(acc[4]); PIN(acc[5]); PIN(acc[6]); PIN(acc[7]); FIN(1);
    mfma16_acc(acc[12], cat(wh[8], wh[9]), xf[4]); SBAR(); wh[6] = lds_trh(wlo1 + 256); SBAR();
    mfma16_acc(acc[13], cat(wh[8], wh[9]), xf[5]); SBAR(); wh[7] = lds_trh(wlo1 + 5376); SBAR();
    mfma16_acc(acc[14], cat(wh[10], wh[11]), xf[4]); SBAR(); wh[8] = lds_trh(wlo0 + 10496); SBAR();
    mfma16_acc(acc[15], cat(wh[10], wh[11]), xf[5]); SBAR(); wh[9] = lds_trh(wlo0 + 15616); SBAR();
    mfma16_acc(acc[12], cat(wh[0], wh[1]), xf[6]); SBAR(); wh[10] = lds_trh(wlo1 + 10496); SBAR();
    mfma16_acc(acc[13], cat(wh[0], wh[1]), xf[7]); SBAR(); wh[11] = lds_trh(wlo1 + 15616); SBAR();
    mfma16_acc(acc[14], cat(wh[2], wh[3]), xf[6]); SBAR();
    mfma16_acc(acc[15], cat(wh[2], wh[3]), xf[7]); SBAR(); PIN(acc[8]); PIN(acc[9]); PIN(acc[10]); PIN(acc[11]); FIN(2);
    mfma16_acc(acc[16], cat(wh[4], wh[5]), xf[4]); SBAR();
    mfma16_acc(acc[17], cat(wh[4], wh[5]), xf[5]); SBAR();
    mfma16_acc(acc[18], cat(wh[6], wh[7]), xf[4]); SBAR();
    mfma16_acc(acc[19], cat(wh[6], wh[7]), xf[5]); SBAR();
    mfma16_acc(acc[16], cat(wh[8], wh[9]), xf[6]); SBAR();
    mfma16_acc(acc[17], cat(wh[8], wh[9]), xf[7]); SBAR();
    mfma16_acc(acc[18], cat(wh[10], wh[11]), xf[6]); SBAR();
    mfma16_acc(acc[19], cat(wh[10], wh[11]), xf[7]); SBAR(); PIN(acc[12]); PIN(acc[13]); PIN(acc[14]); PIN(acc[15]); FIN(3);
    mfma_settle(); PIN(acc[16]); PIN(acc[17]); PIN(acc[18]); PIN(acc[19]); FIN(4);
    ST(12);
    bar();
    if (w != hg) { waitvm<0>(); if (ln == 0) publish(p.ws, flag_off(2, cu, w), tag); }
  } else {
    const unsigned lane = tid & 63;
    const srd_t sX = mk_srd(br ? p.x[1] : p.x[0], X_BYTES), sQ = mk_srd(br ? p.wq[1] : p.wq[0], W_BYTES);
    const unsigned vX0 = (lane >> 3) * 1280 + 16 * ((lane & 7) ^ (lane >> 4)), vX1 = (lane >> 3) * 1280 + 16 * ((lane & 7) ^ (4 + (lane >> 4)));
    unsigned vK[3], vV[3], vQ[3], vR[3];
#pragma unroll
    for (int q = 0; q < 3; ++q) { const unsigned pp = 64 * q + lane, r = pp / 24, c = pp % 24; vQ[q] = 12288 * r + 16 * (c ^ (r & 2)) - 1024 * q; vR[q] = 12288 * r + 16 * (c ^ (r & 2) ^ 4) - 1024 * q; }
    const srd_t sO = mk_srd(br ? p.wo[1] : p.wo[0], W_BYTES);
    unsigned jk = 0;
    const unsigned wsh = 8 * b * 1280 + hh * 320 - (hh & 1) * 64;
    const unsigned vW = lane < 18 ? (((18 * w + lane) / 24) * 64 + ((18 * w + lane) % 24) / 3) * 1280 + ((18 * w + lane) % 3) * 128 : OOB;
#define WO_TOUCH(head) touch(jk, vW, sO, (head) * 192 * 1280 + wsh)
    {
      unsigned gk = HROT == 1 ? ((5 * b + 10 * hh) >> 2) % 10 : HROT == 2 ? (b & 1) * 2 + (hh & 1) * 5 : HROT == 3 ? (b + 2 * hh) % 5 * 2 : HROT == 4 ? ((b + hh) & 1) * 5 : HROT == 5 ? ((b + hh) % 3) * 3 : 0;
      unsigned sx = (b * 128 + 32 * w) * 1280 + 128 * gk, sq = (16 * w + 64 * gk) * 12288 + h0 * 384;
#define HEAD_TILE(buf) do { DMA4X("", L + (buf) + 4096 * w, vX0, vX1, sX, sx); DMA3("", L + (buf) + 16384 + 6144 * w, vQ[0], vQ[1], vQ[2], sQ, sq); \
                            DMA3("", L + (buf) + 19456 + 6144 * w, vR[0], vR[1], vR[2], sQ, sq + 8 * 12288); \
                            sx += gk == 9 ? 128 - 1280 : 128; sq += gk == 9 ? (64 - 640) * 12288 : 64 * 12288; gk = gk == 9 ? 0 : gk + 1; } while (0)
      HEAD_TILE(81920);
      HEAD_TILE(122880);
      ST(19);
      waitvm<10>(); bar(); ST(20);
      HEAD_TILE(0);
      waitvm<10>(); bar(); ST(21);
      HEAD_TILE(40960); HEAD_TILE(81920);
      unsigned nb = 122880;
#pragma nounroll
      for (int g = 2; g < 7; ++g) { waitvm<20>(); bar(); STR(20 + g); HEAD_TILE(nb); nb = nb == 122880 ? 0 : nb + 40960; }
#undef HEAD_TILE
    }
    const srd_t sK = mk_srd(br ? p.k[1] : p.k[0], KV_BYTES), sV = mk_srd(br ? p.v[1] : p.v[0], KV_BYTES);
#pragma unroll
    for (int q = 0; q < 3; ++q) {
      const unsigned pp = 64 * q + lane, r = pp / 24, c = pp % 24;
      vK[q] = 384 * (8 * w + r) + 16 * (c ^ (((8 * w + r) >> 1) & 7)) - 1024 * q;
      vV[q] = 384 * (8 * w + r) + 16 * (c ^ (r & 6)) - 1024 * q;
    }
#define K_TILE(k) DMA3(" nt", L + L_K + 12288 * (k) + 3072 * w, vK[0], vK[1], vK[2], sK, kv0 + 12288 * (k))
#define V_TILE(k) DMA3(" nt", L + L_V + 12288 * (k) + 3072 * w, vV[0], vV[1], vV[2], sV, kv0 + 12288 * (k))
    waitvm<20>(); bar(); ST(27);
    K_TILE(0); K_TILE(1); K_TILE(2);
    waitvm<19>(); bar(); ST(28);
    V_TILE(0); V_TILE(1);
    waitvm<15>(); bar(); ST(29);
    unsigned sx = (b * 128 + 32 * w) * 1280, sq = 16 * w * 12288 + (h0 + 16) * 384, ib = L + 81920;
    int id = 40960;
    auto aux0 = [&]() { DMA4X("", ib + 4096 * w, vX0, vX1, sX, sx); DMA3("", ib + 16384 + 6144 * w, vQ[0], vQ[1], vQ[2], sQ, sq); };
    auto aux1 = [&]() { DMA3("", ib + 19456 + 6144 * w, vR[0], vR[1], vR[2], sQ, sq + 8 * 12288); sx += 128; sq += 64 * 12288; ib += id; id = -id; };
    waitvm<12>(); bar();
    ST(1);
    aux0(); aux1();
    waitvm<13>();
    __builtin_amdgcn_s_setprio(1);
    unsigned kslot = 0, vslot = 24576, kofs = kv0 + 3 * 12288, vofs = kv0 + 2 * 12288;
    unsigned pace0 = 0, pacep = 0;
    auto pace = [&](int i) {
      if (PACE && w == 0) {
        unsigned t = (unsigned)__builtin_amdgcn_s_memrealtime();
        if (i == 0) {
          const unsigned per = PACE >> 4, ph = (j * 8 + xcd) * per >> 8;
          pace0 = t - (t + per - ph) % per;
        }
        else {
          unsigned due = pace0 + ((i <= 32 ? (unsigned)i * PACE : 32u * PACE + (unsigned)(i - 32) * PACE2) >> 4);
          const unsigned pm = (i <= 32 ? PACE : PACE2) * PMIN >> 10;
          if (PMIN && (int)(pacep + pm - due) > 0) due = pacep + pm;
#pragma nounroll
          for (int n = 0; n < 64 && (int)(due - t) > 0; ++n) { __builtin_amdgcn_s_sleep(1); t = (unsigned)__builtin_amdgcn_s_memrealtime(); }
        }
        pacep = t;
      }
    };
#define bar_p(i) do { pace(i); bar(); } while (0)
    auto kv = [&](int i) {
      kofs += i == 29 ? 15 * ITEM : 0;
      vofs += i == 30 ? 15 * ITEM : 0;
      DMA3(" nt", L + L_K + kslot + 3072 * w, vK[0], vK[1], vK[2], sK, kofs);
      DMA3(" nt", L + L_V + vslot + 3072 * w, vV[0], vV[1], vV[2], sV, vofs);
      kofs += 12288; vofs += 12288;
      kslot = kslot == 24576 ? 0 : kslot + 12288; vslot = vslot == 24576 ? 0 : vslot + 12288;
    };
    {
      int cd = 40960;
      unsigned xb[2], wb[4];
#pragma unroll
      for (int k = 0; k < 2; ++k) xb[k] = L + 81920 + x_base(lane, w, k);
#pragma unroll
      for (int e = 0; e < 4; ++e) wb[e] = L + 81920 + 16384 + wq_base(lane, e);
      f32x4 qa[24];
      s16x4 wh[24];
      bf16x8 xf[4];
#pragma unroll
      for (int t = 0; t < 24; ++t) { qa[t] = f32x4{}; PIN(qa[t]); }
      auto flip = [&]() {
        xb[0] += cd; xb[1] += cd;
#pragma unroll
        for (int e = 0; e < 4; ++e) wb[e] += cd;
        cd = -cd;
      };
      auto gemm = [&](const int i0, const int n, const int r0, const int nr, const int xk) {
        SBAR();
#pragma unroll
        for (int i = 0; i < 12; ++i) {
          const int f = i0 + i, r = r0 + i;
          if (i < n) { mfma16_acc(qa[2 * (f % 12)], cat(wh[2 * i], wh[2 * i + 1]), xf[2 * (f / 12)]); mfma16_acc(qa[2 * (f % 12) + 1], cat(wh[2 * i], wh[2 * i + 1]), xf[2 * (f / 12) + 1]); SBAR(); }
          if (i < nr) { const unsigned ad = wb[r & 3] + 128 * ((r % 12) >> 2) + 12288 * (r / 12); wh[2 * i] = lds_trh(ad); wh[2 * i + 1] = lds_trh(ad + 1536); SBAR(); }
        }
        if (xk >= 0) { xf[2 * xk] = lds_rd(xb[xk]); xf[2 * xk + 1] = lds_rd(xb[xk] + 2048); }
        SBAR();
      };
      bar_p(0); kv(0); aux0(); waitvm<13>();
      bar_p(1); kv(1); gemm(0, 0, 0, 12, 0); aux1(); waitvm<16>();
      bar_p(2); kv(2); gemm(0, 12, 12, 12, 1); waitvm<6>();
      bar_p(3); kv(3); flip(); gemm(12, 12, 0, 8, 0); aux0(); waitvm<13>();
#pragma nounroll
      for (int i = 4; i < 25; i += 3) {
        bar_p(i); kv(i); gemm(0, 8, 8, 8, 1); aux1(); waitvm<16>();
        bar_p(i + 1); kv(i + 1); gemm(8, 8, 16, 8, -1); waitvm<6>();
        bar_p(i + 2); kv(i + 2); flip(); gemm(16, 8, 0, 8, 0); aux0(); waitvm<13>();
      }
      bar_p(25); kv(25); gemm(0, 8, 8, 8, 1); aux1(); waitvm<16>();
      bar_p(26); kv(26); gemm(8, 8, 16, 8, -1); waitvm<6>();
      bar_p(27); kv(27); flip(); gemm(16, 8, 0, 8, 0); waitvm<6>();
      bar_p(28); kv(28); gemm(0, 8, 8, 8, 1); waitvm<6>();
      bar_p(29); kv(29); gemm(8, 8, 16, 8, -1); waitvm<6>();
      bar_p(30); kv(30); gemm(16, 8, 0, 0, -1);
      mfma_settle();
#pragma unroll
      for (int t = 0; t < 24; ++t) PIN(qa[t]);
#pragma unroll
      for (int f = 0; f < 12; ++f) lds_wr(L + L_G + 4096 * w + park(f) + 16 * lane, pack_rows(qa[2 * (f & ~1) + (f & 1)] * CS, qa[2 * (f & ~1) + 2 + (f & 1)] * CS));
      waitvm<6>();
    }
    bar_p(31); kv(31); waitvm<6>();
    ST(2);
    unsigned vO[5];
    wo_src(vO, lane);
    const unsigned fo = flag_off(0, cu - hh + ((hh + 1 + lane % 3) & 3), w);
    bf16x8 oa[12];
    u32x2 ep, fl;
    bar_p(32); ld_epoch(ep, sC, WS_EPOCH + 256 * grp); kv(32);
#pragma unroll
    for (int f = 0; f < 12; ++f) oa[f] = lds_rd(L + L_G + 4096 * w + park(f) + 16 * lane);
    waitvm<7>();
    bar_p(33); kv(33); asm volatile("s_waitcnt vmcnt(6)" : "+v"(ep)::"memory");
    tag = mk_tag(ep);
#pragma nounroll
    for (int i = 34; i < 54; ++i) { bar_p(i); kv(i); waitvm<6>(); }
    pace(54);
    bar();
    kv(54);
    WO_TOUCH(h0);
    waitvm<7>();
    pace(55);
    bar();
    kv(55);
    WO_TOUCH(ghead(0));
    waitvm<8>();
    pace(56);
    bar();
    kv(56);
    waitvm<7>();
    pace(57);
    bar();
    kv(57);
    waitvm<6>();
    pace(58);
    bar();
    WO_AT(h0, 0, 122880);
    kv(58);
    WO_TOUCH(ghead(1));
    WO_TOUCH(ghead(2));
    WO_TOUCH(ghead(3));
    waitvm<14>();
    pace(59);
    bar();
    WO_AT(h0, 1, 143360);
    kv(59);
    waitvm<14>();
    pace(60);
    bar();
    kv(60);
    st12_sc1(oa, 16 * lane, sOW, ob + (h0) * 49152);
    waitvm<18>();
    bar();
    DMA3(" nt", L + L_V + vslot + 3072 * w, vV[0], vV[1], vV[2], sV, vofs);
    WO_TOUCH(ghead(4));
    WO_TOUCH(ghead(5));
    WO_TOUCH(ghead(6));
    waitvm<18>();
    bar();
    waitvm<3>();
    if (lane == 0) publish(p.ws, flag_off(0, cu, w), tag);
    bar();
    WO_AT(h0, 2, 0);
    WO_AT(ghead(0), 1, 49152);
    waitvm<41>();
    __builtin_amdgcn_s_setprio(0);
    ST(3);
    bar();
    WO_AT(ghead(0), 0, 20480);
    asm volatile("buffer_load_dwordx2 %0, %1, %2, %3 offen sc1" : "=v"(fl) : "v"(fo), "s"(sC), "s"(0u) : "memory");
    asm volatile("s_waitcnt vmcnt(0)" : "+v"(fl)::"memory");
    if (!__all((((u64)fl[1] << 32) | fl[0]) == tag)) await3(p.ws, fo + 128 * 1024 * 0, tag);
    ST(4);
    bar();
    WO_AT(ghead(0), 2, 122880);
    O_AT(ghead(1), 0, 73728);
    waitvm<20>();
    bar();
    WO_AT(ghead(1), 0, 143360);
    O_AT(ghead(1), 1, 90112);
    waitvm<19>();
    bar();
    WO_AT(ghead(1), 1, 0);
    O_AT(ghead(1), 2, 106496);
    waitvm<33>();
    bar();
    WO_AT(ghead(1), 2, 20480);
    waitvm<27>();
    bar();
    O_AT(ghead(2), 0, 40960);
    O_AT(ghead(2), 1, 57344);
    waitvm<26>();
    ST(5);
    bar();
    WO_AT(ghead(2), 0, 122880);
    waitvm<22>();
    bar();
    WO_AT(ghead(2), 1, 143360);
    O_AT(ghead(2), 2, 73728);
    waitvm<22>();
    bar();
    WO_AT(ghead(2), 2, 0);
    O_AT(ghead(3), 0, 90112);
    waitvm<18>();
    bar();
    asm volatile("buffer_load_dwordx2 %0, %1, %2, %3 offen sc1" : "=v"(fl) : "v"(fo), "s"(sC), "s"(131072u) : "memory");
    WO_AT(ghead(3), 0, 20480);
    O_AT(ghead(3), 1, 106496);
    waitvm<23>();
    bar();
    WO_AT(ghead(3), 1, 122880);
    O_AT(ghead(3), 2, 40960);
    asm volatile("s_waitcnt vmcnt(18)" : "+v"(fl)::"memory");
    if (!__all((((u64)fl[1] << 32) | fl[0]) == tag)) await3(p.ws, fo + 128 * 1024 * 1, tag);
    bar();
    WO_AT(ghead(3), 2, 143360);
    O_AT(ghead(4), 0, 57344);
    waitvm<22>();
    ST(6);
    bar();
    WO_AT(ghead(4), 0, 0);
    O_AT(ghead(4), 1, 73728);
    waitvm<22>();
    bar();
    WO_AT(ghead(4), 1, 20480);
    O_AT(ghead(4), 2, 90112);
    waitvm<22>();
    bar();
    WO_AT(ghead(4), 2, 122880);
    O_AT(ghead(5), 0, 106496);
    waitvm<22>();
    ST(7);
    bar();
    WO_AT(ghead(5), 0, 143360);
    O_AT(ghead(5), 1, 40960);
    waitvm<22>();
    bar();
    WO_AT(ghead(5), 1, 0);
    O_AT(ghead(5), 2, 57344);
    waitvm<22>();
    bar();
    WO_AT(ghead(5), 2, 20480);
    O_AT(ghead(6), 0, 73728);
    waitvm<22>();
    ST(9);
    bar();
    WO_AT(ghead(6), 0, 122880);
    O_AT(ghead(6), 1, 90112);
    waitvm<22>();
    bar();
    WO_AT(ghead(6), 1, 143360);
    O_AT(ghead(6), 2, 106496);
    waitvm<22>();
    bar();
    WO_AT(ghead(6), 2, 0);
    waitvm<18>();
    ST(10);
    bar();
    waitvm<9>();
    bar();
    waitvm<0>();
    bar();
    ST(11);
    PIN(jk);
    bar();
  }

  const unsigned lane = lane_id();
  {
    const srd_t sPW = mk_srd((char*)p.ws + WS_P, P_BYTES);
    const unsigned pgrp = grp * 4 + hh;
    ST(13);
    await3(p.ws, flag_off(2, cu - 4 * hg + 4 * ((hg + 1 + lane % 3) & 3), hg), tag);
    ST(14);
    if (tid == 0 && hg == 0 && hh == 0) publish(p.ws, WS_EPOCH + 256 * grp, tag);
    const srd_t sOut = mk_srd(p.out, OUT_BYTES);
    for (unsigned u = wv; u < 10; u += 8) {
      bf16x8 pr[3];
      const unsigned so = pgrp * 4 * 40960 + 10240 * hg + 1024 * u;
      asm volatile("buffer_load_dwordx4 %0, %3, %4, %5 offen sc1\n\tbuffer_load_dwordx4 %1, %3, %4, %6 offen sc1\n\t"
                   "buffer_load_dwordx4 %2, %3, %4, %7 offen sc1\n\ts_waitcnt vmcnt(0)"
                   : "=&v"(pr[0]), "=&v"(pr[1]), "=&v"(pr[2])
                   : "v"(16 * lane), "s"(sPW), "s"(so + 40960 * ((hg + 1) & 3)), "s"(so + 40960 * ((hg + 2) & 3)), "s"(so + 40960 * ((hg + 3) & 3)) : "memory");
      f32x4 m0 = lds_rdf(L + OWN_BUF + 2048 * u + 16 * lane), m1 = lds_rdf(L + OWN_BUF + 2048 * u + 1024 + 16 * lane);
#pragma unroll
      for (int e = 0; e < 4; ++e) { m0[e] = (m0[e] + (float)pr[0][e]) + ((float)pr[1][e] + (float)pr[2][e]); m1[e] = (m1[e] + (float)pr[0][4 + e]) + ((float)pr[1][4 + e] + (float)pr[2][4 + e]); }
      __builtin_amdgcn_raw_buffer_store_b128(__builtin_bit_cast(u32x4, pack_rows(m0, m1)), sOut, (lane & 15) * 2560 + 16 * (lane >> 4),
                                             (b * 128 + 32 * hg + 16 * (u & 1)) * 2560 + br * 1280 + hh * 320 + 64 * (u >> 1), 0);
    }
  }
}

extern "C" void kernel_launch(void* const* d_in, const int* in_sizes, int n_in, void* d_out, int out_size, void* d_ws, size_t ws_size, hipStream_t stream) {
  Params p;
  for (int i = 0; i < 2; ++i) { p.x[i] = d_in[5 * i]; p.k[i] = d_in[5 * i + 1]; p.v[i] = d_in[5 * i + 2]; p.wq[i] = d_in[5 * i + 3]; p.wo[i] = d_in[5 * i + 4]; }
  p.out = d_out; p.ws = d_ws;
  (void)hipFuncSetAttribute((const void*)mha2_fused, hipFuncAttributeMaxDynamicSharedMemorySize, (int)LDS_BYTES);
  hipLaunchKernelGGL(mha2_fused, dim3(256), dim3(512), LDS_BYTES, stream, p);
}
```
